```python
import torch

def _softmax(x, axis=-1):
    return torch.softmax(x, dim=axis)
def _gelu(x):
    return torch.nn.functional.gelu(x, approximate="tanh")
def _silu(x):
    return torch.nn.functional.silu(x)
def _sigmoid(x):
    return torch.sigmoid(x)
def _layernorm(x, axis=-1, eps=1e-5):
    m = x.mean(dim=axis, keepdim=True)
    v = x.var(dim=axis, keepdim=True, unbiased=False)
    return (x - m) / torch.sqrt(v + eps)
def _scatter_add(tgt, idx, src, axis=0):
    return tgt.clone().index_add_(axis, idx, src)
def _segment_sum(src, seg, n):
    out = torch.zeros((n,) + src.shape[1:], dtype=src.dtype)
    return out.index_add_(0, seg, src)
def _pad(x, pw):
    flat = tuple(v for pair in reversed(pw) for v in pair)
    return torch.nn.functional.pad(x, flat)

kernel_name = 'parallel_sum__sliding_block_cached__dense_activation_relu__decode'

def setup_inputs():

    _w = (lambda a: (lambda t: t.to(torch.bfloat16) if t.is_floating_point() else t)(torch.as_tensor(a)))
    def _A_setup():
        B, Sq, D = 1, 1, 512
        Hq, Hkv, Sk, Dh = 32, 8, 65536, 128
        assert Hq % Hkv == 0, (Hq, Hkv)
        return {
            "x": _w((lambda *s: torch.randn(*s, dtype=torch.bfloat16) * 0.05)(B, Sq, D)),
            "k_cache": _w((lambda *s: torch.randn(*s, dtype=torch.bfloat16) * 3.2)(B, Hkv, Sk, Dh)),
            "v_cache": _w((lambda *s: torch.randn(*s, dtype=torch.bfloat16) * 1.6)(B, Hkv, Sk, Dh)),
            "wq": _w((lambda *s: torch.randn(*s, dtype=torch.bfloat16) * 3.2)(D, Hq * Dh) * (D ** -0.5)),
            "wo": _w((lambda *s: torch.randn(*s, dtype=torch.bfloat16) * 1.6)(Hq * Dh, D) * ((Hq * Dh) ** -0.5)),
        }
    _A_in = _A_setup()

    def _B_setup():
        B, S, D, Dff = 1, 1, 512, 1792
        return {
            "x": _w((lambda *s: torch.randn(*s, dtype=torch.bfloat16) * 0.05)(B, S, D)),
            "w1": _w((lambda *s: torch.randn(*s, dtype=torch.bfloat16) * 0.05)(D, Dff)),
            "w2": _w((lambda *s: torch.randn(*s, dtype=torch.bfloat16) * 0.05)(Dff, D)),
        }
    _B_in = _B_setup()

    return {
        "A_x": _A_in["x"],
        "A_k_cache": _A_in["k_cache"],
        "A_v_cache": _A_in["v_cache"],
        "A_wq": _A_in["wq"],
        "A_wo": _A_in["wo"],
        "B_x": _B_in["x"],
        "B_w1": _B_in["w1"],
        "B_w2": _B_in["w2"],
    }

def reference(A_x, A_k_cache, A_v_cache, A_wq, A_wo, B_x, B_w1, B_w2):
    def _A_ref(x, k_cache, v_cache, wq, wo):
        B, Sq, D = x.shape
        Hq, Dh = 32, 128
        Hkv, Sk = k_cache.shape[1], k_cache.shape[2]
        W = min(4096, max(256, Sk // 4))
        rep = Hq // Hkv
        q = (lambda x, *d: x.permute(*d))((x @ wq).reshape(B, Sq, Hq, Dh), 0, 2, 1, 3)
        k = (lambda x, n, axis: torch.repeat_interleave(x, n, dim=axis))(k_cache[:, :, Sk - W:, :], rep, axis=1)
        v = (lambda x, n, axis: torch.repeat_interleave(x, n, dim=axis))(v_cache[:, :, Sk - W:, :], rep, axis=1)
        scores = torch.einsum("bhqd,bhkd->bhqk", q, k) * (Dh ** -0.5)
        p = _softmax(scores, axis=-1)
        o = torch.einsum("bhqk,bhkd->bhqd", p, v)
        return (lambda x, *d: x.permute(*d))(o, 0, 2, 1, 3).reshape(B, Sq, Hq * Dh) @ wo

    def _B_ref(x, w1, w2):
        h = torch.einsum("bsd,df->bsf", x, w1)
        zero = torch.zeros_like(h)
        h = torch.maximum(h, zero)
        return torch.einsum("bsf,fd->bsd", h, w2)

    x = A_x
    xn = _layernorm(x, axis=-1)
    return x + _A_ref(xn, A_k_cache, A_v_cache, A_wq, A_wo) + _B_ref(xn, B_w1, B_w2)
```

```cpp
#include <hip/hip_runtime.h>
#include <hip/hip_bf16.h>
#include <stdint.h>

typedef unsigned u32;
typedef unsigned long long u64;
typedef unsigned short u16;
typedef __attribute__((ext_vector_type(4))) unsigned u32x4;
typedef __attribute__((ext_vector_type(4))) float f32x4;
typedef __attribute__((ext_vector_type(2))) float f32x2;
typedef __attribute__((ext_vector_type(2))) __bf16 bf16x2;
#define GAS __attribute__((address_space(1)))

namespace {

constexpr int D = 512, HKV = 8, DH = 128, NQ = 4096, DFF = 1792, WIN = 4096;
constexpr int NWG = 256, NT = 512;
constexpr int NRED = 64, CPR = D / NRED;
constexpr size_t WS_Q = 4096;
constexpr size_t WS_ML = WS_Q + 8 * 256 * 8;
constexpr size_t WS_O = WS_ML + 8 * 4 * 32 * 2 * 8;
constexpr size_t WS_F = WS_O + (size_t)8 * 32 * 32 * 16 * 8;
constexpr u32 SPIN_CAP = 1u << 16;

constexpr int L_QRED = 0;
constexpr int L_QS = 128;
constexpr int L_HRED = 384;
constexpr int L_ORED = 448;
constexpr int L_MML = 576;
constexpr int L_FRED = 640;
constexpr int L_BIG = 704;
constexpr int L_TOTAL = L_BIG + 4096;

__device__ __forceinline__ float bfr(float f) { return (float)(__bf16)f; }
__device__ __forceinline__ float lo16(u32 u) { return __uint_as_float(u << 16); }
__device__ __forceinline__ float hi16(u32 u) { return __uint_as_float(u & 0xffff0000u); }
__device__ __forceinline__ u32 bfbits(float f) { return (u32)__builtin_bit_cast(u16, (__bf16)f); }
__device__ __forceinline__ float dot2(u32 k, u32 q, float acc) {
  return __builtin_amdgcn_fdot2_f32_bf16(__builtin_bit_cast(bf16x2, k), __builtin_bit_cast(bf16x2, q), acc, false);
}
template <int CTRL> __device__ __forceinline__ float dpp(float x) {
  return __builtin_bit_cast(float, __builtin_amdgcn_mov_dpp(__builtin_bit_cast(int, x), CTRL, 0xf, 0xf, true));
}
constexpr int XOR1 = 0xB1, XOR2 = 0x4E, XOR7 = 0x141, XOR8 = 0x128, ROR4 = 0x124;
__device__ __forceinline__ float xrow_max(float x) {
  auto s = __builtin_amdgcn_permlane16_swap(__float_as_uint(x), __float_as_uint(x), false, false);
  x = fmaxf(__uint_as_float(s[0]), __uint_as_float(s[1]));
  auto t = __builtin_amdgcn_permlane32_swap(__float_as_uint(x), __float_as_uint(x), false, false);
  return fmaxf(__uint_as_float(t[0]), __uint_as_float(t[1]));
}
__device__ __forceinline__ float xrow_sum(float x) {
  auto s = __builtin_amdgcn_permlane16_swap(__float_as_uint(x), __float_as_uint(x), false, false);
  x = __uint_as_float(s[0]) + __uint_as_float(s[1]);
  auto t = __builtin_amdgcn_permlane32_swap(__float_as_uint(x), __float_as_uint(x), false, false);
  return __uint_as_float(t[0]) + __uint_as_float(t[1]);
}
__device__ __forceinline__ float wave_sum(float v) {
  v += dpp<XOR1>(v); v += dpp<XOR2>(v); v += dpp<XOR7>(v); v += dpp<XOR8>(v);
  return xrow_sum(v);
}
__device__ __forceinline__ u64 ld_agent(const GAS u64* p) { return __hip_atomic_load(p, __ATOMIC_RELAXED, __HIP_MEMORY_SCOPE_AGENT); }
__device__ __forceinline__ void st_agent(GAS u64* p, u64 v) { __hip_atomic_store(p, v, __ATOMIC_RELAXED, __HIP_MEMORY_SCOPE_AGENT); }
__device__ __forceinline__ u64 pack40(float v, u32 tlo, u32 thi8) {
  const u32 b = __float_as_uint(v);
  u32 r = ((b & 0x7fffffffu) > 0x7f800000u) ? (b | 0x00400000u) : (b + 0x7fu + ((b >> 8) & 1u));
  r = (r & 0xffffff00u) | thi8;
  return ((u64)r << 32) | tlo;
}
__device__ __forceinline__ bool ok40(u64 g, u32 tlo, u32 thi8) { return (u32)g == tlo && ((u32)(g >> 32) & 0xffu) == thi8; }
__device__ __forceinline__ float val40(u64 g) { return __uint_as_float((u32)(g >> 32) & 0xffffff00u); }
#define PIN() do { asm volatile("" ::: "memory"); __builtin_amdgcn_sched_barrier(0); } while (0)

__global__ void __launch_bounds__(NT, 2)
fused_decode_kernel(const u16* x_, const u16* kc_, const u16* vc_, const u16* wq_, const u16* wo_,
                    const u16* w1_, const u16* w2_, u16* out_, unsigned char* ws_, int Sk) {
  __shared__ __attribute__((aligned(16))) float lds[L_TOTAL];
  const GAS u16* x = (const GAS u16*)x_;
  const int tid = threadIdx.x, lane = tid & 63, wave = __builtin_amdgcn_readfirstlane(tid >> 6);
  const int b = blockIdx.x, g = b & 7, j = b >> 3;
  const int r = lane >> 4, c = lane & 15;
  GAS unsigned char* ws = (GAS unsigned char*)ws_;
  const bool has_ffn = j < 28;
  const int f = g * 28 + j;

  const u32x4 xv = *(const GAS u32x4*)(x + 8 * lane);
  const int row0 = tid >> 1;
  const u32 xa = x[row0], xb = x[row0 + 256], xc = x[tid];
  const u64 E = ld_agent((const GAS u64*)ws);
  const GAS u16* wqp = (const GAS u16*)wq_ + (size_t)row0 * NQ + g * 512 + j * 16 + (tid & 1) * 8;
  const u32x4 wqa = *(const GAS u32x4*)wqp;
  const u32x4 wqb = *(const GAS u32x4*)(wqp + (size_t)256 * NQ);
  PIN();
  __builtin_amdgcn_s_barrier();
  PIN();
  const size_t kvoff = ((size_t)g * Sk + (Sk - WIN) + j * 128 + wave * 16 + r) * DH + 8 * c;
  u32x4 Kc[4], Vc[4];
#pragma unroll
  for (int t = 0; t < 4; ++t) Kc[t] = __builtin_nontemporal_load((const GAS u32x4*)((const GAS u16*)kc_ + kvoff + (size_t)t * 4 * DH));
  PIN();
#pragma unroll
  for (int t = 0; t < 4; ++t) Vc[t] = __builtin_nontemporal_load((const GAS u32x4*)((const GAS u16*)vc_ + kvoff + (size_t)t * 4 * DH));
  PIN();

  float xf[8];
#pragma unroll
  for (int k = 0; k < 4; ++k) { xf[2 * k] = lo16(xv[k]); xf[2 * k + 1] = hi16(xv[k]); }
  float s = 0.f;
#pragma unroll
  for (int k = 0; k < 8; ++k) s += xf[k];
  const float mean = wave_sum(s) * (1.f / D);
  float ss = 0.f;
#pragma unroll
  for (int k = 0; k < 8; ++k) { const float d = xf[k] - mean; ss = fmaf(d, d, ss); }
  const float var = wave_sum(ss) * (1.f / D);
  const float mb = bfr(mean);
  const float den = bfr(sqrtf(bfr(bfr(var) + 1e-5f)));
  auto ln = [&](u32 xr) { return bfr(bfr(lo16(xr) - mb) / den); };
  const u64 tagE = E + 1;
  const u32 tlo = (u32)tagE, thi8 = (u32)(tagE >> 32) & 0xffu;

  {
    const float xna = ln(xa), xnb = ln(xb);
    float v[8];
#pragma unroll
    for (int k = 0; k < 4; ++k) {
      v[2 * k] = fmaf(xna, lo16(wqa[k]), xnb * lo16(wqb[k]));
      v[2 * k + 1] = fmaf(xna, hi16(wqa[k]), xnb * hi16(wqb[k]));
    }
    { const bool up = lane & 2;
#pragma unroll
      for (int i = 0; i < 4; ++i) { const float send = up ? v[i] : v[i + 4], keep = up ? v[i + 4] : v[i]; v[i] = keep + __shfl_xor(send, 2); } }
    { const bool up = lane & 4;
#pragma unroll
      for (int i = 0; i < 2; ++i) { const float send = up ? v[i] : v[i + 2], keep = up ? v[i + 2] : v[i]; v[i] = keep + __shfl_xor(send, 4); } }
    { const bool up = lane & 8; const float send = up ? v[0] : v[1], keep = up ? v[1] : v[0]; v[0] = keep + __shfl_xor(send, 8); }
    v[0] = xrow_sum(v[0]);
    if (lane < 16) lds[L_QRED + wave * 16 + (lane & 1) * 8 + ((lane >> 1) & 1) * 4 + ((lane >> 2) & 1) * 2 + ((lane >> 3) & 1)] = v[0];
  }
  __syncthreads();
  if (wave == 0 && lane < 8) {
    float a0 = 0.f, a1 = 0.f;
#pragma unroll
    for (int w = 0; w < 8; ++w) { const f32x2 t = *(const f32x2*)&lds[L_QRED + w * 16 + 2 * lane]; a0 += t.x; a1 += t.y; }
    const u32 pr = bfbits(a0) | (bfbits(a1) << 16);
    st_agent((GAS u64*)(ws + WS_Q) + g * 256 + j * 8 + lane, ((u64)tlo << 32) | pr);
  }
  PIN();
  asm volatile("s_waitcnt vmcnt(4)" ::: "memory");
  PIN();
  const GAS u64* qg_p = (const GAS u64*)(ws + WS_Q) + g * 256 + wave * 32 + (lane & 31);
  u64 qg = ld_agent(qg_p);
  PIN();
  u32x4 w1v = {0u, 0u, 0u, 0u}, w2v = {0u, 0u, 0u, 0u};
  if (has_ffn) {
    w1v = *(const GAS u32x4*)((const GAS u16*)w1_ + (size_t)tid * DFF + f * 8);
    w2v = __builtin_nontemporal_load((const GAS u32x4*)((const GAS u16*)w2_ + (size_t)(f * 8 + wave) * D + 8 * lane));
  }
  const GAS u16* wop = (const GAS u16*)wo_ + (size_t)((g * 32 + j) * 16 + wave) * D + 8 * lane;
  const u32x4 woa = __builtin_nontemporal_load((const GAS u32x4*)wop);
  const u32x4 wob = __builtin_nontemporal_load((const GAS u32x4*)(wop + 8 * D));
  PIN();
  for (u32 spins = 0; !__all((u32)(qg >> 32) == tlo) && spins < SPIN_CAP; ++spins) {
    __builtin_amdgcn_s_sleep(1);
    qg = ld_agent(qg_p);
  }
  if (lane < 32) ((u32*)lds)[L_QS + wave * 32 + lane] = (u32)qg;
  __syncthreads();

  {
    u32x4 qpk[4];
#pragma unroll
    for (int h = 0; h < 4; ++h) qpk[h] = *(const u32x4*)&((const u32*)lds)[L_QS + h * 64 + 4 * c];
    float xs[16];
#pragma unroll
    for (int t = 0; t < 4; ++t)
#pragma unroll
      for (int h = 0; h < 4; ++h) {
        float d = 0.f;
#pragma unroll
        for (int k = 0; k < 4; ++k) d = dot2(Kc[t][k], qpk[h][k], d);
        xs[t * 4 + h] = d;
      }
#define TR_STEP(HALF, CTRL, BIT) _Pragma("unroll") for (int h = 0; h < (HALF); ++h) { \
      const float lo_ = xs[h], hi_ = xs[h + (HALF)]; const float t1_ = lo_ + dpp<CTRL>(lo_), t2_ = hi_ + dpp<CTRL>(hi_); \
      xs[h] = (c & (BIT)) ? t2_ : t1_; }
    TR_STEP(8, XOR8, 8) TR_STEP(4, XOR7, 4) TR_STEP(2, XOR2, 2) TR_STEP(1, XOR1, 1)
#undef TR_STEP
    const float sc = bfr(bfr(xs[0]) * 0.08838834764831845f);
    float m = sc;
    m = fmaxf(m, dpp<ROR4>(m)); m = fmaxf(m, dpp<XOR8>(m)); m = xrow_max(m);
    const float p = __builtin_amdgcn_exp2f((sc - m) * 1.4426950408889634f);
    float l = p;
    l += dpp<ROR4>(l); l += dpp<XOR8>(l); l = xrow_sum(l);
    float acc[32];
#pragma unroll
    for (int k = 0; k < 32; ++k) acc[k] = 0.f;
#define PV_T(T) { float vf[8]; \
      _Pragma("unroll") for (int k = 0; k < 4; ++k) { vf[2 * k] = lo16(Vc[T][k]); vf[2 * k + 1] = hi16(Vc[T][k]); } \
      const float p0 = dpp<0x150 + 4 * T + 0>(p), p1 = dpp<0x150 + 4 * T + 1>(p), p2 = dpp<0x150 + 4 * T + 2>(p), p3 = dpp<0x150 + 4 * T + 3>(p); \
      _Pragma("unroll") for (int k = 0; k < 8; ++k) { acc[k] = fmaf(p0, vf[k], acc[k]); acc[8 + k] = fmaf(p1, vf[k], acc[8 + k]); \
        acc[16 + k] = fmaf(p2, vf[k], acc[16 + k]); acc[24 + k] = fmaf(p3, vf[k], acc[24 + k]); } }
    PV_T(0) PV_T(1) PV_T(2) PV_T(3)
#undef PV_T
#pragma unroll
    for (int i = 0; i < 16; ++i) {
      auto t = __builtin_amdgcn_permlane32_swap(__float_as_uint(acc[i]), __float_as_uint(acc[i + 16]), false, false);
      acc[i] = __uint_as_float(t[0]) + __uint_as_float(t[1]);
    }
#pragma unroll
    for (int i = 0; i < 8; ++i) {
      auto t = __builtin_amdgcn_permlane16_swap(__float_as_uint(acc[i]), __float_as_uint(acc[i + 8]), false, false);
      acc[i] = __uint_as_float(t[0]) + __uint_as_float(t[1]);
    }
    float* dst = &lds[L_BIG + wave * 512 + r * 128 + 8 * c];
    *(f32x4*)dst = f32x4{acc[0], acc[1], acc[2], acc[3]};
    *(f32x4*)(dst + 4) = f32x4{acc[4], acc[5], acc[6], acc[7]};
    if (lane < 4) *(f32x2*)&lds[L_MML + (wave * 4 + lane) * 2] = f32x2{m, l};
  }
  __syncthreads();
  {
    const int hh = tid >> 7;
    float mw[8], M = -INFINITY;
#pragma unroll
    for (int w = 0; w < 8; ++w) { mw[w] = lds[L_MML + (w * 4 + hh) * 2]; M = fmaxf(M, mw[w]); }
    float o = 0.f, L = 0.f;
#pragma unroll
    for (int w = 0; w < 8; ++w) {
      const float e = __builtin_amdgcn_exp2f((mw[w] - M) * 1.4426950408889634f);
      o = fmaf(e, lds[L_BIG + w * 512 + tid], o);
      L = fmaf(e, lds[L_MML + (w * 4 + hh) * 2 + 1], L);
    }
    st_agent((GAS u64*)(ws + WS_O) + ((size_t)(g * 32 + (tid >> 4)) * 32 + j) * 16 + (tid & 15), pack40(o, tlo, thi8));
    if ((tid & 127) < 2)
      st_agent((GAS u64*)(ws + WS_ML) + ((g * 4 + hh) * 32 + j) * 2 + (tid & 1), pack40((tid & 1) ? L : M, tlo, thi8));
  }
  PIN();
  const GAS u64* og_p = (const GAS u64*)(ws + WS_O) + (size_t)(g * 32 + j) * 512 + tid;
  const GAS u64* mg_p = (const GAS u64*)(ws + WS_ML) + (g * 4 + (j >> 3)) * 64 + lane;
  u64 og = ld_agent(og_p), mg = ld_agent(mg_p);
  PIN();
  if (has_ffn) {
    const float xn = ln(xc);
    float v[8];
#pragma unroll
    for (int k = 0; k < 4; ++k) { v[2 * k] = xn * lo16(w1v[k]); v[2 * k + 1] = xn * hi16(w1v[k]); }
    { const bool up = lane & 1;
#pragma unroll
      for (int i = 0; i < 4; ++i) { const float send = up ? v[i] : v[i + 4], keep = up ? v[i + 4] : v[i]; v[i] = keep + __shfl_xor(send, 1); } }
    { const bool up = lane & 2;
#pragma unroll
      for (int i = 0; i < 2; ++i) { const float send = up ? v[i] : v[i + 2], keep = up ? v[i + 2] : v[i]; v[i] = keep + __shfl_xor(send, 2); } }
    { const bool up = lane & 4; const float send = up ? v[0] : v[1], keep = up ? v[1] : v[0]; v[0] = keep + __shfl_xor(send, 4); }
    v[0] += __shfl_xor(v[0], 8);
    v[0] = xrow_sum(v[0]);
    if (lane < 8) lds[L_HRED + wave * 8 + (lane & 1) * 4 + ((lane >> 1) & 1) * 2 + ((lane >> 2) & 1)] = v[0];
  }
  for (u32 spins = 0; !__all(ok40(og, tlo, thi8) && ok40(mg, tlo, thi8)) && spins < SPIN_CAP; ++spins) {
    __builtin_amdgcn_s_sleep(1);
    og = ld_agent(og_p); mg = ld_agent(mg_p);
  }
  float Ltot;
  {
    const float mv = val40(mg);
    const float ot = dpp<XOR1>(mv);
    const float ms = (lane & 1) ? ot : mv, ls = (lane & 1) ? mv : ot;
    float M = ms;
    M = fmaxf(M, dpp<XOR2>(M)); M = fmaxf(M, dpp<XOR7>(M)); M = fmaxf(M, dpp<XOR8>(M)); M = xrow_max(M);
    const float wgt = __builtin_amdgcn_exp2f((ms - M) * 1.4426950408889634f);
    Ltot = wave_sum((lane & 1) ? wgt * ls : 0.f);
    const float mine = __shfl(wgt, 2 * (4 * wave + r));
    const float ov = xrow_sum(mine * val40(og));
    if (lane < 16) lds[L_ORED + wave * 16 + lane] = ov;
  }
  __syncthreads();
  {
    float oa = 0.f, ob = 0.f, hv = 0.f;
#pragma unroll
    for (int w = 0; w < 8; ++w) { oa += lds[L_ORED + w * 16 + wave]; ob += lds[L_ORED + w * 16 + wave + 8]; }
    oa = bfr(oa / Ltot); ob = bfr(ob / Ltot);
    if (has_ffn) {
#pragma unroll
      for (int w = 0; w < 8; ++w) hv += lds[L_HRED + w * 8 + wave];
      hv = fmaxf(bfr(hv), 0.f);
    }
    float y[8];
#pragma unroll
    for (int k = 0; k < 4; ++k) {
      y[2 * k] = fmaf(oa, lo16(woa[k]), fmaf(ob, lo16(wob[k]), hv * lo16(w2v[k])));
      y[2 * k + 1] = fmaf(oa, hi16(woa[k]), fmaf(ob, hi16(wob[k]), hv * hi16(w2v[k])));
    }
    float* dst = &lds[L_BIG + wave * 512 + 8 * lane];
    *(f32x4*)dst = f32x4{y[0], y[1], y[2], y[3]};
    *(f32x4*)(dst + 4) = f32x4{y[4], y[5], y[6], y[7]};
  }
  __syncthreads();
  {
    float y = 0.f;
#pragma unroll
    for (int w = 0; w < 8; ++w) y += lds[L_BIG + w * 512 + tid];
    st_agent((GAS u64*)(ws + WS_F) + ((size_t)(tid / CPR) * 256 + b) * CPR + (tid % CPR), pack40(y, tlo, thi8));
  }
  if (b >= NRED) return;
  {
    const GAS u64* fp = (const GAS u64*)(ws + WS_F) + (size_t)b * 256 * CPR + tid * 4;
    u64 fg[4];
    for (u32 spins = 0;; ++spins) {
      bool ok = true;
#pragma unroll
      for (int k = 0; k < 4; ++k) { fg[k] = ld_agent(fp + k); ok = ok && ok40(fg[k], tlo, thi8); }
      if (__all(ok) || spins >= SPIN_CAP) break;
      __builtin_amdgcn_s_sleep(1);
    }
    float v[4];
#pragma unroll
    for (int k = 0; k < 4; ++k) v[k] = val40(fg[k]);
    { const bool up = lane & 2;
#pragma unroll
      for (int i = 0; i < 2; ++i) { const float send = up ? v[i] : v[i + 2], keep = up ? v[i + 2] : v[i]; v[i] = keep + __shfl_xor(send, 2); } }
    { const bool up = lane & 4; const float send = up ? v[0] : v[1], keep = up ? v[1] : v[0]; v[0] = keep + __shfl_xor(send, 4); }
    v[0] += __shfl_xor(v[0], 8);
    v[0] = xrow_sum(v[0]);
    if (lane < 8) lds[L_FRED + wave * 8 + (lane & 1) * 4 + ((lane >> 1) & 1) * 2 + ((lane >> 2) & 1)] = v[0];
    __syncthreads();
    if (tid < CPR) {
      float y = 0.f;
#pragma unroll
      for (int w = 0; w < 8; ++w) y += lds[L_FRED + w * 8 + tid];
      const int n = b * CPR + tid;
      ((GAS u16*)out_)[n] = (u16)bfbits(lo16(x[n]) + y);
    }
    if (b == 0 && tid == 0) st_agent((GAS u64*)ws, tagE);
  }
}

}

extern "C" void kernel_launch(void* const* d_in, const int* in_sizes, int n_in,
                              void* d_out, int out_size, void* d_ws, size_t ws_size,
                              hipStream_t stream) {
  const u16* x = (const u16*)d_in[0];
  const u16* kc = (const u16*)d_in[1];
  const u16* vc = (const u16*)d_in[2];
  const u16* wq = (const u16*)d_in[3];
  const u16* wo = (const u16*)d_in[4];
  const u16* w1 = (const u16*)d_in[6];
  const u16* w2 = (const u16*)d_in[7];
  const int Sk = in_sizes[1] / (HKV * DH);
  fused_decode_kernel<<<NWG, NT, 0, stream>>>(x, kc, vc, wq, wo, w1, w2, (u16*)d_out, (unsigned char*)d_ws, Sk);
}
```

```cpp
#include <hip/hip_runtime.h>
#include <hip/hip_bf16.h>
#include <stdint.h>

typedef unsigned u32;
typedef unsigned long long u64;
typedef unsigned short u16;
typedef __attribute__((ext_vector_type(4))) unsigned u32x4;
typedef __attribute__((ext_vector_type(2))) unsigned u32x2;
typedef __attribute__((ext_vector_type(4))) float f32x4;
typedef __attribute__((ext_vector_type(2))) float f32x2;
typedef __attribute__((ext_vector_type(8))) short s16x8;
typedef __attribute__((ext_vector_type(4))) short s16x4;
typedef __attribute__((ext_vector_type(2))) __bf16 bf16x2;
typedef __attribute__((ext_vector_type(8))) __bf16 bf16x8;
#define GAS __attribute__((address_space(1)))
#define LAS __attribute__((address_space(3)))
#define EARLY_W 0

namespace {

constexpr int D = 512, HKV = 8, DH = 128, NQ = 4096, DFF = 1792, WIN = 4096;
constexpr int NWG = 256, NT = 512;
constexpr int NRED = 64, CPR = D / NRED;
constexpr size_t WS_Q = 4096;
constexpr size_t WS_ML = WS_Q + 8 * 32 * 256;
constexpr size_t WS_H = WS_ML + 8 * 4 * 32 * 128;
constexpr size_t WS_O = WS_H + 32 * 8 * 8 * 128;
constexpr size_t WS_F = WS_O + (size_t)8 * 32 * 32 * 16 * 8;
constexpr size_t WS_END = WS_F + (size_t)256 * 512 * 8;
constexpr u32 SPIN_CAP = 1u << 16;
constexpr float LOG2E = 1.4426950408889634f;

constexpr int L_VT = 0;
constexpr int L_OW = 32768;
constexpr int L_QS = 40960;
constexpr int L_QRED = 46080;
constexpr int L_WML = 42240;
constexpr int L_ORED = 42496;
constexpr int L_FRED = 42752;
constexpr int L_HV = 42400;
constexpr int L_CNT = 42880;
constexpr int L_XN = 42912;
constexpr int L_HRED = 44960;
constexpr int L_TOTAL = 46080 + 512;

__device__ __forceinline__ float bfr(float f) { return (float)(__bf16)f; }
__device__ __forceinline__ float lo16(u32 u) { return __uint_as_float(u << 16); }
__device__ __forceinline__ float hi16(u32 u) { return __uint_as_float(u & 0xffff0000u); }
__device__ __forceinline__ u32 bfbits(float f) { return (u32)__builtin_bit_cast(u16, (__bf16)f); }
__device__ __forceinline__ u32 pk2(float a, float b) { bf16x2 v = {(__bf16)a, (__bf16)b}; return __builtin_bit_cast(u32, v); }
template <int CTRL> __device__ __forceinline__ float dpp(float x) {
  return __builtin_bit_cast(float, __builtin_amdgcn_mov_dpp(__builtin_bit_cast(int, x), CTRL, 0xf, 0xf, true));
}
constexpr int XOR1 = 0xB1, XOR2 = 0x4E, XOR7 = 0x141, XOR8 = 0x128;
__device__ __forceinline__ float xrow_max(float x) {
  auto s = __builtin_amdgcn_permlane16_swap(__float_as_uint(x), __float_as_uint(x), false, false);
  x = fmaxf(__uint_as_float(s[0]), __uint_as_float(s[1]));
  auto t = __builtin_amdgcn_permlane32_swap(__float_as_uint(x), __float_as_uint(x), false, false);
  return fmaxf(__uint_as_float(t[0]), __uint_as_float(t[1]));
}
__device__ __forceinline__ float xrow_sum(float x) {
  auto s = __builtin_amdgcn_permlane16_swap(__float_as_uint(x), __float_as_uint(x), false, false);
  x = __uint_as_float(s[0]) + __uint_as_float(s[1]);
  auto t = __builtin_amdgcn_permlane32_swap(__float_as_uint(x), __float_as_uint(x), false, false);
  return __uint_as_float(t[0]) + __uint_as_float(t[1]);
}
__device__ __forceinline__ float wave_sum(float v) {
  v += dpp<XOR1>(v); v += dpp<XOR2>(v); v += dpp<XOR7>(v); v += dpp<XOR8>(v);
  return xrow_sum(v);
}
__device__ __forceinline__ float tr32(float a, float b) {
  auto t = __builtin_amdgcn_permlane32_swap(__float_as_uint(a), __float_as_uint(b), false, false);
  return __uint_as_float(t[0]) + __uint_as_float(t[1]);
}
__device__ __forceinline__ float tr16(float a, float b) {
  auto t = __builtin_amdgcn_permlane16_swap(__float_as_uint(a), __float_as_uint(b), false, false);
  return __uint_as_float(t[0]) + __uint_as_float(t[1]);
}
template <int CTRL> __device__ __forceinline__ float trd(float a, float b, bool up) {
  const float send = up ? a : b, keep = up ? b : a;
  return keep + dpp<CTRL>(send);
}
__device__ __forceinline__ float tr8_hi(const float* v, int lane) {
  const float a0 = tr32(v[0], v[4]), a1 = tr32(v[1], v[5]), a2 = tr32(v[2], v[6]), a3 = tr32(v[3], v[7]);
  const float b0 = tr16(a0, a2), b1 = tr16(a1, a3);
  return trd<XOR8>(b0, b1, lane & 8);
}
__device__ __forceinline__ float sel4(f32x4 a, int i) {
  const float lo = (i & 1) ? a[1] : a[0], hi = (i & 1) ? a[3] : a[2];
  return (i & 2) ? hi : lo;
}
__device__ __forceinline__ f32x4 mfma16(u32x4 a, u32x4 b, f32x4 c) {
  return __builtin_amdgcn_mfma_f32_16x16x32_bf16(__builtin_bit_cast(bf16x8, a), __builtin_bit_cast(bf16x8, b), c, 0, 0, 0);
}
__device__ __forceinline__ u64 ld_agent(const GAS u64* p) { return __hip_atomic_load(p, __ATOMIC_RELAXED, __HIP_MEMORY_SCOPE_AGENT); }
__device__ __forceinline__ void st_agent(GAS u64* p, u64 v) { __hip_atomic_store(p, v, __ATOMIC_RELAXED, __HIP_MEMORY_SCOPE_AGENT); }
__device__ __forceinline__ u64 pack40(float v, u32 tlo, u32 thi8) {
  const u32 b = __float_as_uint(v);
  u32 r = ((b & 0x7fffffffu) > 0x7f800000u) ? (b | 0x00400000u) : (b + 0x7fu + ((b >> 8) & 1u));
  r = (r & 0xffffff00u) | thi8;
  return ((u64)r << 32) | tlo;
}
__device__ __forceinline__ u32 bad40(u64 g, u32 tlo, u32 thi8) { return ((u32)g ^ tlo) | (((u32)(g >> 32) ^ thi8) & 0xffu); }
__device__ __forceinline__ float val40(u32 hi) { return __uint_as_float(hi & 0xffffff00u); }
__device__ __forceinline__ u32 voff(u32 row, u32 ch) { return 256u * row + 16u * (ch ^ (((row & 3u) << 2) | ((row >> 2) & 3u))); }
#define PIN() do { asm volatile("" ::: "memory"); __builtin_amdgcn_sched_barrier(0); } while (0)
#define STAMP(i)
#define STAMP_FLUSH()

#define GAP 20
#define D2 30
#define D3 30
#define GAP0 24
#define PSLEEP 0
#define LDNT(p) __builtin_nontemporal_load((const GAS u32x4*)(p))
__device__ __forceinline__ u32 lds_ld(const unsigned char* p) { return __hip_atomic_load((const u32*)p, __ATOMIC_RELAXED, __HIP_MEMORY_SCOPE_WORKGROUP); }
__device__ __forceinline__ void lds_wait_ge(const unsigned char* p, u32 want) {
  for (u32 spins = 0; (u32)__builtin_amdgcn_readfirstlane(lds_ld(p)) < want && spins < (1u << 22); ++spins) __builtin_amdgcn_s_sleep(1);
  asm volatile("" ::: "memory");
}
__global__ void __launch_bounds__(NT, 2)
fused_decode_kernel(const u16* x_, const u16* kc_, const u16* vc_, const u16* wq_, const u16* wo_,
                    const u16* w1_, const u16* w2_, u16* out_, unsigned char* ws_, int Sk) {
  __shared__ __attribute__((aligned(16))) unsigned char lds[L_TOTAL];
  const GAS u16* x = (const GAS u16*)x_;
  const int tid = threadIdx.x, lane = tid & 63, wave = __builtin_amdgcn_readfirstlane(tid >> 6);
  const int b = blockIdx.x, g = b & 7, j = b >> 3;
  const int r = lane >> 4, c = lane & 15;
  GAS unsigned char* ws = (GAS unsigned char*)ws_;
  const bool has_ffn = j < 28;
  const int jf = has_ffn ? j : 0;
  const int wv = wave & 3, t = tid & 255;
  STAMP(0);
  if (wave >= 4) {
    u32x4 R[6];
    const u32x4 xv = *(const GAS u32x4*)(x + 8 * lane);
    const u64 E = ld_agent((const GAS u64*)ws);
    const GAS u16* wqp = (const GAS u16*)wq_ + (size_t)(256 * (j & 1) + (t >> 2)) * NQ + g * 512 + (j >> 1) * 32 + (t & 3) * 8;
#pragma unroll
    for (int i = 0; i < 4; ++i) R[i] = LDNT(wqp + (size_t)64 * i * NQ);
    const GAS u16* w1p = (const GAS u16*)w1_ + (size_t)(64 * g + (t >> 3)) * DFF + 64 * jf + 8 * (t & 7);
    R[4] = LDNT(w1p); R[5] = LDNT(w1p + (size_t)32 * DFF);
    PIN();
    __builtin_amdgcn_s_barrier();
    PIN();
    float xf[8];
#pragma unroll
    for (int k = 0; k < 4; ++k) { xf[2 * k] = lo16(xv[k]); xf[2 * k + 1] = hi16(xv[k]); }
    float s = 0.f;
#pragma unroll
    for (int k = 0; k < 8; ++k) s += xf[k];
    const float mean = wave_sum(s) * (1.f / D);
    float ss = 0.f;
#pragma unroll
    for (int k = 0; k < 8; ++k) { const float d = xf[k] - mean; ss = fmaf(d, d, ss); }
    const float var = wave_sum(ss) * (1.f / D);
    const float mb = bfr(mean);
    const float rden = __builtin_amdgcn_rcpf(bfr(__builtin_amdgcn_sqrtf(bfr(bfr(var) + 1e-5f))));
#pragma unroll
    for (int k = 0; k < 8; ++k) xf[k] = bfr(bfr(xf[k] - mb) * rden);
    *(f32x4*)(lds + L_XN + lane * 32) = f32x4{xf[0], xf[1], xf[2], xf[3]};
    *(f32x4*)(lds + L_XN + lane * 32 + 16) = f32x4{xf[4], xf[5], xf[6], xf[7]};
    const u64 tagE = E + 1;
    const u32 tlo = (u32)tagE, thi8 = (u32)(tagE >> 32) & 0xffu;
    STAMP(1);
    {
      float v[8];
#pragma unroll
      for (int k = 0; k < 8; ++k) v[k] = 0.f;
#pragma unroll
      for (int i = 0; i < 4; ++i) {
        const float xn = *(const float*)(lds + L_XN + (256 * (j & 1) + (t >> 2) + 64 * i) * 4);
#pragma unroll
        for (int k = 0; k < 4; ++k) { v[2 * k] = fmaf(xn, lo16(R[i][k]), v[2 * k]); v[2 * k + 1] = fmaf(xn, hi16(R[i][k]), v[2 * k + 1]); }
      }
      float q = tr8_hi(v, lane);
      q += __builtin_bit_cast(float, __builtin_amdgcn_ds_swizzle(__builtin_bit_cast(int, q), 0x101F));
      if ((lane & 4) == 0) {
        const int col = (lane & 3) * 8 + (lane >> 5) * 4 + ((lane >> 4) & 1) * 2 + ((lane >> 3) & 1);
        *(float*)(lds + L_QRED + (col * 4 + wv) * 4) = q;
      }
    }
    {
      const float xa = *(const float*)(lds + L_XN + (64 * g + (t >> 3)) * 4), xb = *(const float*)(lds + L_XN + (64 * g + 32 + (t >> 3)) * 4);
      float v[8];
#pragma unroll
      for (int k = 0; k < 4; ++k) { v[2 * k] = fmaf(xa, lo16(R[4][k]), xb * lo16(R[5][k])); v[2 * k + 1] = fmaf(xa, hi16(R[4][k]), xb * hi16(R[5][k])); }
      const float h = tr8_hi(v, lane);
      *(float*)(lds + L_HRED + ((8 * (lane & 7) + (lane >> 5) * 4 + ((lane >> 4) & 1) * 2 + ((lane >> 3) & 1)) * 4 + wv) * 4) = h;
    }
    STAMP(2);
    asm volatile("s_waitcnt lgkmcnt(0)" ::: "memory");
    u32 old = 0;
    if (lane == 0) old = __hip_atomic_fetch_add((u32*)(lds + L_CNT), 1u, __ATOMIC_RELAXED, __HIP_MEMORY_SCOPE_WORKGROUP);
    old = __builtin_amdgcn_readfirstlane(old);
    if (old == 3u) {
      asm volatile("" ::: "memory");
      const f32x4 a = *(const f32x4*)(lds + L_QRED + (lane & 31) * 16);
      if (lane < 32) st_agent((GAS u64*)(ws + WS_Q) + (g * 32 + j) * 32 + lane, pack40((a.x + a.y) + (a.z + a.w), tlo, thi8));
      const f32x4 hh = *(const f32x4*)(lds + L_HRED + lane * 16);
      if (has_ffn) st_agent((GAS u64*)(ws + WS_H) + ((j * 8 + (lane >> 3)) * 8 + g) * 16 + (lane & 7), pack40((hh.x + hh.y) + (hh.z + hh.w), tlo, thi8));
      PIN();
      if (lane == 0) __hip_atomic_store((u32*)(lds + L_CNT + 24), 1u, __ATOMIC_RELAXED, __HIP_MEMORY_SCOPE_WORKGROUP);
    }
    PIN();
    STAMP(3);
    lds_wait_ge(lds + L_CNT + 24, 1u);
    {
      const __amdgpu_buffer_rsrc_t rs = __builtin_amdgcn_make_buffer_rsrc((void*)(ws_ + WS_Q), 0, 8 * 32 * 256, 0x00020000);
      const int off = (g * 32 + (t >> 4) * 2) * 256 + (t & 15) * 16;
      u32x4 a[1], bq[1];
#define QPOLL(k) do { asm volatile("" ::: "memory"); a[k] = __builtin_amdgcn_raw_buffer_load_b128(rs, off, 0, 16); bq[k] = __builtin_amdgcn_raw_buffer_load_b128(rs, off + 256, 0, 16); PIN(); } while (0)
#define QBAD(v) ((v.x ^ tlo) | ((v.y ^ thi8) & 0xffu) | (v.z ^ tlo) | ((v.w ^ thi8) & 0xffu))
#define QOK(k) __all((QBAD(a[k]) | QBAD(bq[k])) == 0)
#define QDONE(k) do { qpk = pk2(val40(a[k].y) + val40(bq[k].y), val40(a[k].w) + val40(bq[k].w)); } while (0)
      u32 qpk = 0;
      lds_wait_ge(lds + L_CNT + 16, 4u);
      for (u32 spins = 0;; ++spins) {
        QPOLL(0);
        if (QOK(0) || spins >= SPIN_CAP) { QDONE(0); break; }
      }
      *(u32*)(lds + L_QS + t * 4) = qpk;
      asm volatile("s_waitcnt lgkmcnt(0)" ::: "memory");
      if (lane == 0) __hip_atomic_fetch_add((u32*)(lds + L_CNT + 4), 1u, __ATOMIC_RELAXED, __HIP_MEMORY_SCOPE_WORKGROUP);
    }
    PIN();
    STAMP(4);
    STAMP(5);
    STAMP_FLUSH();
    return;
  }
  if (tid < 8) *(u32*)(lds + L_CNT + tid * 4) = 0u;
  const u64 E = ld_agent((const GAS u64*)ws);
  asm volatile("s_waitcnt lgkmcnt(0)" ::: "memory");
  PIN();
  __builtin_amdgcn_s_barrier();
  PIN();
  u32x4 Kf[8], Vc[8];
  const size_t kvbase = ((size_t)g * Sk + (Sk - WIN) + j * 128 + wv * 32) * DH;
  u32x4 Wo[4], W2[2];
  {
    const GAS u16* w2p = (const GAS u16*)w2_ + (size_t)(64 * jf + 8 * g + r) * D + 128 * wv + 8 * c;
    const GAS u16* wop = (const GAS u16*)wo_ + (size_t)((g * 32 + j) * 16 + r) * D + 128 * wv + 8 * c;
#pragma unroll
    for (int i = 0; i < 4; ++i) Wo[i] = LDNT(wop + 4 * i * D);
    W2[0] = LDNT(w2p); W2[1] = LDNT(w2p + 4 * D);
  }
  u32 tch = 0, tch2 = 0;
  if (lane == 0) { tch = *(const GAS u32*)((const GAS u16*)kc_ + kvbase); tch2 = *(const GAS u32*)((const GAS u16*)vc_ + kvbase); }
  lds_wait_ge(lds + L_CNT + 24, 1u);
  PIN();
#pragma unroll
  for (int i = 0; i < 8; ++i) Kf[i] = LDNT((const GAS u16*)kc_ + kvbase + ((i >> 2) * 16 + c) * DH + 32 * (i & 3) + 8 * r);
  PIN();
#pragma unroll
  for (int i = 0; i < 8; ++i) Vc[i] = LDNT((const GAS u16*)vc_ + kvbase + (4 * i + r) * DH + 8 * c);
  PIN();
  if (lane == 0) __hip_atomic_fetch_add((u32*)(lds + L_CNT + 16), 1u, __ATOMIC_RELAXED, __HIP_MEMORY_SCOPE_WORKGROUP);
  const GAS u64* hp = (const GAS u64*)(ws + WS_H) + ((jf * 8 + g) * 8 + (lane >> 3)) * 16 + (lane & 7);
  u64 hg = ld_agent(hp);
  PIN();
  STAMP(1);
  const u64 tagE = E + 1;
  const u32 tlo = (u32)tagE, thi8 = (u32)(tagE >> 32) & 0xffu;
  asm volatile("" :: "v"(tch), "v"(tch2));
#pragma unroll
  for (int i = 0; i < 8; ++i) *(u32x4*)(lds + L_VT + voff(32 * wv + 4 * i + r, c)) = Vc[i];
  u32x4 Vf[8];
  {
    const int q4 = c >> 2, p4 = c & 3;
#pragma unroll
    for (int n = 0; n < 8; ++n) {
      const u32x2 w0 = __builtin_bit_cast(u32x2, __builtin_amdgcn_ds_read_tr16_b64_v4i16((LAS s16x4*)(lds + L_VT + voff(32 * wv + 4 * r + q4, 2 * n + (p4 >> 1)) + 8 * (p4 & 1))));
      const u32x2 w1 = __builtin_bit_cast(u32x2, __builtin_amdgcn_ds_read_tr16_b64_v4i16((LAS s16x4*)(lds + L_VT + voff(32 * wv + 16 + 4 * r + q4, 2 * n + (p4 >> 1)) + 8 * (p4 & 1))));
      Vf[n] = u32x4{w0.x, w0.y, w1.x, w1.y};
    }
  }
  float y[8];
  {
    if (has_ffn) {
      for (u32 spins = 0; !__all(bad40(hg, tlo, thi8) == 0) && spins < SPIN_CAP; ++spins) hg = ld_agent(hp);
    }
    float hv = has_ffn ? val40((u32)(hg >> 32)) : 0.f;
    hv += dpp<XOR8>(hv); hv = xrow_sum(hv);
    hv = fmaxf(bfr(hv), 0.f);
    const float h0 = __shfl(hv, r), h1 = __shfl(hv, r + 4);
#pragma unroll
    for (int k = 0; k < 4; ++k) {
      y[2 * k] = fmaf(h0, lo16(W2[0][k]), h1 * lo16(W2[1][k]));
      y[2 * k + 1] = fmaf(h0, hi16(W2[0][k]), h1 * hi16(W2[1][k]));
    }
  }
  PIN();
  STAMP(2);
  lds_wait_ge(lds + L_CNT + 4, 4u);
  STAMP(3);
  {
    f32x4 a0 = {0.f, 0.f, 0.f, 0.f}, a1 = {0.f, 0.f, 0.f, 0.f};
#pragma unroll
    for (int i = 0; i < 4; ++i) {
      const u32x4 qf = *(const u32x4*)(lds + L_QS + (c & 3) * 256 + 64 * i + 16 * r);
      a0 = mfma16(Kf[i], qf, a0);
      a1 = mfma16(Kf[4 + i], qf, a1);
    }
    float sc[8];
#pragma unroll
    for (int i = 0; i < 4; ++i) { sc[i] = bfr(bfr(a0[i]) * 0.08838834764831845f); sc[4 + i] = bfr(bfr(a1[i]) * 0.08838834764831845f); }
    const float mw = xrow_max(fmaxf(fmaxf(fmaxf(sc[0], sc[1]), fmaxf(sc[2], sc[3])), fmaxf(fmaxf(sc[4], sc[5]), fmaxf(sc[6], sc[7]))));
#pragma unroll
    for (int i = 0; i < 8; ++i) sc[i] = __builtin_amdgcn_exp2f((sc[i] - mw) * LOG2E);
    const u32x4 pf = {pk2(sc[0], sc[1]), pk2(sc[2], sc[3]), pk2(sc[4], sc[5]), pk2(sc[6], sc[7])};
    const u32x4 ones = {0x3f803f80u, 0x3f803f80u, 0x3f803f80u, 0x3f803f80u};
    const f32x4 z = {0.f, 0.f, 0.f, 0.f};
    float ow[8];
#pragma unroll
    for (int n = 0; n < 8; ++n) ow[n] = sel4(mfma16(pf, Vf[n], z), r);
    const f32x4 lac = mfma16(pf, ones, z);
    *(f32x4*)(lds + L_OW + ((wv * 4 + r) * 16 + c) * 32) = f32x4{ow[0], ow[1], ow[2], ow[3]};
    *(f32x4*)(lds + L_OW + ((wv * 4 + r) * 16 + c) * 32 + 16) = f32x4{ow[4], ow[5], ow[6], ow[7]};
    if (lane < 4) { *(float*)(lds + L_WML + (lane * 4 + wv) * 4) = mw; *(float*)(lds + L_WML + 64 + (lane * 4 + wv) * 4) = sel4(lac, lane); }
  }
  STAMP(4);
  __syncthreads();
  STAMP(5);
  {
    const f32x4 m4 = *(const f32x4*)(lds + L_WML + r * 16), l4 = *(const f32x4*)(lds + L_WML + 64 + r * 16);
    f32x2 pv[4];
#pragma unroll
    for (int w = 0; w < 4; ++w) pv[w] = *(const f32x2*)(lds + L_OW + ((w * 4 + r) * 16 + c) * 32 + wv * 8);
    const float M = fmaxf(fmaxf(m4.x, m4.y), fmaxf(m4.z, m4.w));
    float e[4];
#pragma unroll
    for (int w = 0; w < 4; ++w) e[w] = __builtin_amdgcn_exp2f((m4[w] - M) * LOG2E);
    const float L = fmaf(e[0], l4.x, fmaf(e[1], l4.y, fmaf(e[2], l4.z, e[3] * l4.w)));
    const float rl = __builtin_amdgcn_rcpf(L);
    const float x0 = fmaf(e[0], pv[0].x, fmaf(e[1], pv[1].x, fmaf(e[2], pv[2].x, e[3] * pv[3].x))) * rl;
    const float x1 = fmaf(e[0], pv[0].y, fmaf(e[1], pv[1].y, fmaf(e[2], pv[2].y, e[3] * pv[3].y))) * rl;
    const float y0 = dpp<XOR1>(x0), y1 = dpp<XOR1>(x1);
    const bool odd = c & 1;
    const u32 pr = odd ? pk2(y1, x1) : pk2(x0, y0);
    const u64 lg = pack40(fmaf(M, LOG2E, __builtin_amdgcn_logf(L)), tlo, thi8);
    const __amdgpu_buffer_rsrc_t ro = __builtin_amdgcn_make_buffer_rsrc((void*)(ws_ + WS_O), 0, (int)(256 * 512 * 8), 0x00020000);
    __builtin_amdgcn_raw_buffer_store_b128(u32x4{pr, tlo, (u32)lg, (u32)(lg >> 32)}, ro, ((g * 32 + r * 8 + 2 * wv + (c & 1)) * 32 + j) * 128 + (c >> 1) * 16, 0, 16);
  }
  PIN();
  STAMP(6);
  {
    const __amdgpu_buffer_rsrc_t ro = __builtin_amdgcn_make_buffer_rsrc((void*)(ws_ + WS_O), 0, (int)(256 * 512 * 8), 0x00020000);
    const int off = (g * 32 + j) * 4096 + t * 16;
    u32x4 og;
    if (D2) __builtin_amdgcn_s_sleep(D2);
    for (u32 spins = 0;; ++spins) {
      asm volatile("" ::: "memory");
      og = __builtin_amdgcn_raw_buffer_load_b128(ro, off, 0, 16);
      const u32 bad = (og.y ^ tlo) | (og.z ^ tlo) | ((og.w ^ thi8) & 0xffu);
      if (__all(bad == 0) || spins >= SPIN_CAP) break;
    }
    STAMP(7);
    const float lv = val40(og.w);
    float Mw = lv;
    Mw = fmaxf(Mw, dpp<XOR8>(Mw)); Mw = xrow_max(Mw);
    const float mine = __builtin_amdgcn_exp2f(lv - Mw);
    float Ww = mine;
    Ww += dpp<XOR8>(Ww); Ww = xrow_sum(Ww);
    float v = tr32(mine * lo16(og.x), mine * hi16(og.x));
    { auto tt = __builtin_amdgcn_permlane16_swap(__float_as_uint(v), __float_as_uint(v), false, false); v = __uint_as_float(tt[0]) + __uint_as_float(tt[1]); }
    v += dpp<XOR8>(v);
    if ((lane & 24) == 0) *(float*)(lds + L_ORED + ((2 * (lane & 7) + (lane >> 5)) * 4 + wv) * 4) = v;
    if (lane == 8) { *(float*)(lds + L_HV + wv * 4) = Mw; *(float*)(lds + L_HV + 16 + wv * 4) = Ww; }
    __syncthreads();
    const f32x4 m4 = *(const f32x4*)(lds + L_HV), w4 = *(const f32x4*)(lds + L_HV + 16);
    const float Mx = fmaxf(fmaxf(m4.x, m4.y), fmaxf(m4.z, m4.w));
    f32x4 e4;
#pragma unroll
    for (int w = 0; w < 4; ++w) e4[w] = __builtin_amdgcn_exp2f(m4[w] - Mx);
    const float rL = __builtin_amdgcn_rcpf(fmaf(e4.x, w4.x, fmaf(e4.y, w4.y, fmaf(e4.z, w4.z, e4.w * w4.w))));
#pragma unroll
    for (int i = 0; i < 4; ++i) {
      const f32x4 a0 = *(const f32x4*)(lds + L_ORED + (r + 4 * i) * 16);
      const float o = bfr(fmaf(e4.x, a0.x, fmaf(e4.y, a0.y, fmaf(e4.z, a0.z, e4.w * a0.w))) * rL);
#pragma unroll
      for (int k = 0; k < 4; ++k) { y[2 * k] = fmaf(o, lo16(Wo[i][k]), y[2 * k]); y[2 * k + 1] = fmaf(o, hi16(Wo[i][k]), y[2 * k + 1]); }
    }
    const float a0 = tr32(y[0], y[4]), a1 = tr32(y[1], y[5]), a2 = tr32(y[2], y[6]), a3 = tr32(y[3], y[7]);
    const float b0 = tr16(a0, a2), b1 = tr16(a1, a3);
    const u64 g0 = pack40(b0, tlo, thi8), g1 = pack40(b1, tlo, thi8);
    const __amdgpu_buffer_rsrc_t rf = __builtin_amdgcn_make_buffer_rsrc((void*)(ws_ + WS_F), 0, (int)(256 * 512 * 8), 0x00020000);
    const int offf = (((16 * wv + c) * 256 + b) * CPR + (lane >> 5) * 4 + ((lane >> 4) & 1) * 2) * 8;
    __builtin_amdgcn_raw_buffer_store_b128(u32x4{(u32)g0, (u32)(g0 >> 32), (u32)g1, (u32)(g1 >> 32)}, rf, offf, 0, 16);
  }
  PIN();
  STAMP(10);
  if (b >= NRED) { STAMP_FLUSH(); return; }
  {
    const __amdgpu_buffer_rsrc_t rs = __builtin_amdgcn_make_buffer_rsrc((void*)(ws_ + WS_F), 0, (int)(256 * 512 * 8), 0x00020000);
    const int off = b * 16384 + tid * 16;
    const u32 xo = x[b * CPR + (tid & 7)];
    if (D3) __builtin_amdgcn_s_sleep(D3);
    u32x4 fg[4];
    for (u32 spins = 0;; ++spins) {
      asm volatile("" ::: "memory");
#pragma unroll
      for (int k = 0; k < 4; ++k) fg[k] = __builtin_amdgcn_raw_buffer_load_b128(rs, off + 4096 * k, 0, 16);
      u32 bad = 0;
#pragma unroll
      for (int k = 0; k < 4; ++k) bad |= (fg[k].x ^ tlo) | ((fg[k].y ^ thi8) & 0xffu) | (fg[k].z ^ tlo) | ((fg[k].w ^ thi8) & 0xffu);
      if (__all(bad == 0) || spins >= SPIN_CAP) break;
    }
    STAMP(8);
    const float s0 = (val40(fg[0].y) + val40(fg[1].y)) + (val40(fg[2].y) + val40(fg[3].y));
    const float s1 = (val40(fg[0].w) + val40(fg[1].w)) + (val40(fg[2].w) + val40(fg[3].w));
    float s = tr32(s0, s1);
    { auto tt = __builtin_amdgcn_permlane16_swap(__float_as_uint(s), __float_as_uint(s), false, false); s = __uint_as_float(tt[0]) + __uint_as_float(tt[1]); }
    s += dpp<XOR8>(s);
    s += __builtin_bit_cast(float, __builtin_amdgcn_ds_swizzle(__builtin_bit_cast(int, s), 0x101F));
    if ((lane & 28) == 0) *(float*)(lds + L_FRED + ((2 * (lane & 3) + (lane >> 5)) * 4 + wave) * 4) = s;
    __syncthreads();
    if (tid < CPR) {
      const f32x4 a = *(const f32x4*)(lds + L_FRED + tid * 16);
      ((GAS u16*)out_)[b * CPR + tid] = (u16)bfbits(lo16(xo) + ((a.x + a.y) + (a.z + a.w)));
    }
    if (b == 0 && tid == 0) st_agent((GAS u64*)ws, tagE);
    STAMP(9);
    STAMP_FLUSH();
  }
}

}

extern "C" void kernel_launch(void* const* d_in, const int* in_sizes, int n_in,
                              void* d_out, int out_size, void* d_ws, size_t ws_size,
                              hipStream_t stream) {
  const u16* x = (const u16*)d_in[0];
  const u16* kc = (const u16*)d_in[1];
  const u16* vc = (const u16*)d_in[2];
  const u16* wq = (const u16*)d_in[3];
  const u16* wo = (const u16*)d_in[4];
  const u16* w1 = (const u16*)d_in[6];
  const u16* w2 = (const u16*)d_in[7];
  const int Sk = in_sizes[1] / (HKV * DH);
  fused_decode_kernel<<<NWG, NT, 0, stream>>>(x, kc, vc, wq, wo, w1, w2, (u16*)d_out, (unsigned char*)d_ws, Sk);
}
```

```cpp
#include <hip/hip_runtime.h>
#include <hip/hip_bf16.h>
#include <stdint.h>

typedef unsigned u32;
typedef unsigned long long u64;
typedef unsigned short u16;
typedef __attribute__((ext_vector_type(4))) unsigned u32x4;
typedef __attribute__((ext_vector_type(2))) unsigned u32x2;
typedef __attribute__((ext_vector_type(4))) float f32x4;
typedef __attribute__((ext_vector_type(2))) float f32x2;
typedef __attribute__((ext_vector_type(8))) short s16x8;
typedef __attribute__((ext_vector_type(4))) short s16x4;
typedef __attribute__((ext_vector_type(2))) __bf16 bf16x2;
typedef __attribute__((ext_vector_type(8))) __bf16 bf16x8;
#define GAS __attribute__((address_space(1)))
#define LAS __attribute__((address_space(3)))
#define EARLY_W 0

namespace {

constexpr int D = 512, HKV = 8, DH = 128, NQ = 4096, DFF = 1792, WIN = 4096;
constexpr int NWG = 256, NT = 512;
constexpr int NRED = 64, CPR = D / NRED;
constexpr size_t WS_Q = 4096;
constexpr size_t WS_ML = WS_Q + 8 * 32 * 256;
constexpr size_t WS_H = WS_ML + 8 * 4 * 32 * 128;
constexpr size_t WS_O = WS_H + 32 * 8 * 8 * 128;
constexpr size_t WS_F = WS_O + (size_t)8 * 32 * 32 * 16 * 8;
constexpr size_t WS_END = WS_F + (size_t)256 * 512 * 8;
constexpr u32 SPIN_CAP = 1u << 16;
constexpr float LOG2E = 1.4426950408889634f;

constexpr int L_VT = 0;
constexpr int L_OW = 32768;
constexpr int L_QS = 40960;
constexpr int L_QRED = 46080;
constexpr int L_WML = 42240;
constexpr int L_ORED = 42496;
constexpr int L_FRED = 42752;
constexpr int L_HV = 42400;
constexpr int L_CNT = 42880;
constexpr int L_XN = 42912;
constexpr int L_HRED = 44960;
constexpr int L_KT = 46592;
constexpr int L_TOTAL = L_KT + 32768;

__device__ __forceinline__ float bfr(float f) { return (float)(__bf16)f; }
__device__ __forceinline__ float lo16(u32 u) { return __uint_as_float(u << 16); }
__device__ __forceinline__ float hi16(u32 u) { return __uint_as_float(u & 0xffff0000u); }
__device__ __forceinline__ u32 bfbits(float f) { return (u32)__builtin_bit_cast(u16, (__bf16)f); }
__device__ __forceinline__ u32 pk2(float a, float b) { bf16x2 v = {(__bf16)a, (__bf16)b}; return __builtin_bit_cast(u32, v); }
template <int CTRL> __device__ __forceinline__ float dpp(float x) {
  return __builtin_bit_cast(float, __builtin_amdgcn_mov_dpp(__builtin_bit_cast(int, x), CTRL, 0xf, 0xf, true));
}
constexpr int XOR1 = 0xB1, XOR2 = 0x4E, XOR7 = 0x141, XOR8 = 0x128;
__device__ __forceinline__ float xrow_max(float x) {
  auto s = __builtin_amdgcn_permlane16_swap(__float_as_uint(x), __float_as_uint(x), false, false);
  x = fmaxf(__uint_as_float(s[0]), __uint_as_float(s[1]));
  auto t = __builtin_amdgcn_permlane32_swap(__float_as_uint(x), __float_as_uint(x), false, false);
  return fmaxf(__uint_as_float(t[0]), __uint_as_float(t[1]));
}
__device__ __forceinline__ float xrow_sum(float x) {
  auto s = __builtin_amdgcn_permlane16_swap(__float_as_uint(x), __float_as_uint(x), false, false);
  x = __uint_as_float(s[0]) + __uint_as_float(s[1]);
  auto t = __builtin_amdgcn_permlane32_swap(__float_as_uint(x), __float_as_uint(x), false, false);
  return __uint_as_float(t[0]) + __uint_as_float(t[1]);
}
__device__ __forceinline__ float wave_sum(float v) {
  v += dpp<XOR1>(v); v += dpp<XOR2>(v); v += dpp<XOR7>(v); v += dpp<XOR8>(v);
  return xrow_sum(v);
}
__device__ __forceinline__ float tr32(float a, float b) {
  auto t = __builtin_amdgcn_permlane32_swap(__float_as_uint(a), __float_as_uint(b), false, false);
  return __uint_as_float(t[0]) + __uint_as_float(t[1]);
}
__device__ __forceinline__ float tr16(float a, float b) {
  auto t = __builtin_amdgcn_permlane16_swap(__float_as_uint(a), __float_as_uint(b), false, false);
  return __uint_as_float(t[0]) + __uint_as_float(t[1]);
}
template <int CTRL> __device__ __forceinline__ float trd(float a, float b, bool up) {
  const float send = up ? a : b, keep = up ? b : a;
  return keep + dpp<CTRL>(send);
}
__device__ __forceinline__ float tr8_hi(const float* v, int lane) {
  const float a0 = tr32(v[0], v[4]), a1 = tr32(v[1], v[5]), a2 = tr32(v[2], v[6]), a3 = tr32(v[3], v[7]);
  const float b0 = tr16(a0, a2), b1 = tr16(a1, a3);
  return trd<XOR8>(b0, b1, lane & 8);
}
__device__ __forceinline__ float sel4(f32x4 a, int i) {
  const float lo = (i & 1) ? a[1] : a[0], hi = (i & 1) ? a[3] : a[2];
  return (i & 2) ? hi : lo;
}
__device__ __forceinline__ f32x4 mfma16(u32x4 a, u32x4 b, f32x4 c) {
  return __builtin_amdgcn_mfma_f32_16x16x32_bf16(__builtin_bit_cast(bf16x8, a), __builtin_bit_cast(bf16x8, b), c, 0, 0, 0);
}
__device__ __forceinline__ u64 ld_agent(const GAS u64* p) { return __hip_atomic_load(p, __ATOMIC_RELAXED, __HIP_MEMORY_SCOPE_AGENT); }
__device__ __forceinline__ void st_agent(GAS u64* p, u64 v) { __hip_atomic_store(p, v, __ATOMIC_RELAXED, __HIP_MEMORY_SCOPE_AGENT); }
__device__ __forceinline__ u64 pack40(float v, u32 tlo, u32 thi8) {
  const u32 b = __float_as_uint(v);
  const u32 rr = b + 0x7fu + ((b >> 8) & 1u), rn = b | 0x00400000u;
  const u32 m = (v != v) ? 0xffffffffu : 0u;
  u32 r = (rn & m) | (rr & ~m);
  r = (r & 0xffffff00u) | thi8;
  return ((u64)r << 32) | tlo;
}
__device__ __forceinline__ u32 bad40(u64 g, u32 tlo, u32 thi8) { return ((u32)g ^ tlo) | (((u32)(g >> 32) ^ thi8) & 0xffu); }
__device__ __forceinline__ float val40(u32 hi) { return __uint_as_float(hi & 0xffffff00u); }
__device__ __forceinline__ u32 voff(u32 row, u32 ch) { return 256u * row + 16u * (ch ^ (((row & 3u) << 2) | ((row >> 2) & 3u))); }
#define PIN() do { asm volatile("" ::: "memory"); __builtin_amdgcn_sched_barrier(0); } while (0)
#define STAMP(i)
#define STAMP_FLUSH()

#define GAP 20
#define WLATE 0
#define KEARLY 0
#define CODE_PF 64
#define D2 30
#define D3 30
#define GAP0 24
#define PSLEEP 0
#define AUXS 18
#define AUXQ 18
#define MKRS(p, n) __builtin_amdgcn_make_buffer_rsrc((void*)(p), 0, (int)(n), 0x00020000)
#define LDB(rs, off, aux) __builtin_amdgcn_raw_buffer_load_b128(rs, (int)(off), 0, aux)
__device__ __forceinline__ u32 lds_ld(const unsigned char* p) { return __hip_atomic_load((const u32*)p, __ATOMIC_RELAXED, __HIP_MEMORY_SCOPE_WORKGROUP); }
__device__ __forceinline__ void lds_spin_ge(const unsigned char* p, u32 want) {
  for (u32 spins = 0; (u32)__builtin_amdgcn_readfirstlane(lds_ld(p)) < want && spins < (1u << 24); ++spins) { }
  asm volatile("" ::: "memory");
}
__device__ __forceinline__ void lds_wait_ge(const unsigned char* p, u32 want) {
  for (u32 spins = 0; (u32)__builtin_amdgcn_readfirstlane(lds_ld(p)) < want && spins < (1u << 22); ++spins) __builtin_amdgcn_s_sleep(1);
  asm volatile("" ::: "memory");
}
__global__ void __launch_bounds__(NT, 2)
fused_decode_kernel(const u16* x_, const u16* kc_, const u16* vc_, const u16* wq_, const u16* wo_,
                    const u16* w1_, const u16* w2_, u16* out_, unsigned char* ws_, int Sk) {
  __shared__ __attribute__((aligned(16))) unsigned char lds[L_TOTAL];
  const GAS u16* x = (const GAS u16*)x_;
  const int tid = threadIdx.x, lane = tid & 63, wave = __builtin_amdgcn_readfirstlane(tid >> 6);
  const int b = blockIdx.x, g = b & 7, j = b >> 3;
  const int r = lane >> 4, c = lane & 15;
  GAS unsigned char* ws = (GAS unsigned char*)ws_;
  const bool has_ffn = j < 28;
  const int jf = has_ffn ? j : 0;
  const int wv = wave & 3, t = tid & 255;
  STAMP(0);
  u32 cpf0 = 0, cpf1 = 0;
  if (wave == 0) {
    const GAS u32* cp = (const GAS u32*)(__builtin_amdgcn_s_getpc() & ~127ull);
    cpf0 = cp[lane * 32];
    if (lane < CODE_PF - 64) cpf1 = cp[(64 + lane) * 32];
  }
  if (wave >= 4) {
    u32x4 R[6];
    const u32x4 xv = *(const GAS u32x4*)(x + 8 * lane);
    const u64 E = ld_agent((const GAS u64*)ws);
    const __amdgpu_buffer_rsrc_t rq = MKRS(wq_, D * NQ * 2), r1 = MKRS(w1_, D * DFF * 2);
    const int qoff = ((256 * (j & 1) + (t >> 2)) * NQ + g * 512 + (j >> 1) * 32 + (t & 3) * 8) * 2;
#pragma unroll
    for (int i = 0; i < 4; ++i) R[i] = LDB(rq, qoff + 64 * i * NQ * 2, AUXQ);
    const int o1 = ((64 * g + (t >> 3)) * DFF + 64 * jf + 8 * (t & 7)) * 2;
    R[4] = LDB(r1, o1, AUXS); R[5] = LDB(r1, o1 + 32 * DFF * 2, AUXS);
    PIN();
    __builtin_amdgcn_s_barrier();
    PIN();
    float xf[8];
#pragma unroll
    for (int k = 0; k < 4; ++k) { xf[2 * k] = lo16(xv[k]); xf[2 * k + 1] = hi16(xv[k]); }
    float s = 0.f;
#pragma unroll
    for (int k = 0; k < 8; ++k) s += xf[k];
    const float mean = wave_sum(s) * (1.f / D);
    float ss = 0.f;
#pragma unroll
    for (int k = 0; k < 8; ++k) { const float d = xf[k] - mean; ss = fmaf(d, d, ss); }
    const float var = wave_sum(ss) * (1.f / D);
    const float mb = bfr(mean);
    const float rden = __builtin_amdgcn_rcpf(bfr(__builtin_amdgcn_sqrtf(bfr(bfr(var) + 1e-5f))));
#pragma unroll
    for (int k = 0; k < 8; ++k) xf[k] = bfr(bfr(xf[k] - mb) * rden);
    *(f32x4*)(lds + L_XN + lane * 32) = f32x4{xf[0], xf[1], xf[2], xf[3]};
    *(f32x4*)(lds + L_XN + lane * 32 + 16) = f32x4{xf[4], xf[5], xf[6], xf[7]};
    const u64 tagE = E + 1;
    const u32 tlo = (u32)tagE, thi8 = (u32)(tagE >> 32) & 0xffu;
    STAMP(1);
    {
      float v[8];
#pragma unroll
      for (int k = 0; k < 8; ++k) v[k] = 0.f;
#pragma unroll
      for (int i = 0; i < 4; ++i) {
        const float xn = *(const float*)(lds + L_XN + (256 * (j & 1) + (t >> 2) + 64 * i) * 4);
#pragma unroll
        for (int k = 0; k < 4; ++k) { v[2 * k] = fmaf(xn, lo16(R[i][k]), v[2 * k]); v[2 * k + 1] = fmaf(xn, hi16(R[i][k]), v[2 * k + 1]); }
      }
      float q = tr8_hi(v, lane);
      q += __builtin_bit_cast(float, __builtin_amdgcn_ds_swizzle(__builtin_bit_cast(int, q), 0x101F));
      if ((lane & 4) == 0) {
        const int col = (lane & 3) * 8 + (lane >> 5) * 4 + ((lane >> 4) & 1) * 2 + ((lane >> 3) & 1);
        *(float*)(lds + L_QRED + (col * 4 + wv) * 4) = q;
      }
    }
    {
      const float xa = *(const float*)(lds + L_XN + (64 * g + (t >> 3)) * 4), xb = *(const float*)(lds + L_XN + (64 * g + 32 + (t >> 3)) * 4);
      float v[8];
#pragma unroll
      for (int k = 0; k < 4; ++k) { v[2 * k] = fmaf(xa, lo16(R[4][k]), xb * lo16(R[5][k])); v[2 * k + 1] = fmaf(xa, hi16(R[4][k]), xb * hi16(R[5][k])); }
      const float h = tr8_hi(v, lane);
      *(float*)(lds + L_HRED + ((8 * (lane & 7) + (lane >> 5) * 4 + ((lane >> 4) & 1) * 2 + ((lane >> 3) & 1)) * 4 + wv) * 4) = h;
    }
    STAMP(2);
    asm volatile("s_waitcnt lgkmcnt(0)" ::: "memory");
    u32 old = 0;
    if (lane == 0) old = __hip_atomic_fetch_add((u32*)(lds + L_CNT), 1u, __ATOMIC_RELAXED, __HIP_MEMORY_SCOPE_WORKGROUP);
    old = __builtin_amdgcn_readfirstlane(old);
    if (old == 3u) {
      asm volatile("" ::: "memory");
      const f32x4 a = *(const f32x4*)(lds + L_QRED + (lane & 31) * 16);
      if (lane < 32) st_agent((GAS u64*)(ws + WS_Q) + (g * 32 + j) * 32 + lane, pack40((a.x + a.y) + (a.z + a.w), tlo, thi8));
      const f32x4 hh = *(const f32x4*)(lds + L_HRED + lane * 16);
      if (has_ffn) st_agent((GAS u64*)(ws + WS_H) + ((j * 8 + (lane >> 3)) * 8 + g) * 16 + (lane & 7), pack40((hh.x + hh.y) + (hh.z + hh.w), tlo, thi8));
      PIN();
      if (lane == 0) __hip_atomic_store((u32*)(lds + L_CNT + 24), 1u, __ATOMIC_RELAXED, __HIP_MEMORY_SCOPE_WORKGROUP);
    }
    PIN();
    STAMP(3);
    lds_wait_ge(lds + L_CNT + 24, 1u);
    {
      const __amdgpu_buffer_rsrc_t rs = __builtin_amdgcn_make_buffer_rsrc((void*)(ws_ + WS_Q), 0, 8 * 32 * 256, 0x00020000);
      const int off = (g * 32 + (t >> 4) * 2) * 256 + (t & 15) * 16;
      u32x4 a[1], bq[1];
#define QPOLL(k) do { asm volatile("" ::: "memory"); a[k] = __builtin_amdgcn_raw_buffer_load_b128(rs, off, 0, 16); bq[k] = __builtin_amdgcn_raw_buffer_load_b128(rs, off + 256, 0, 16); PIN(); } while (0)
#define QBAD(v) ((v.x ^ tlo) | ((v.y ^ thi8) & 0xffu) | (v.z ^ tlo) | ((v.w ^ thi8) & 0xffu))
#define QOK(k) __all((QBAD(a[k]) | QBAD(bq[k])) == 0)
#define QDONE(k) do { qpk = pk2(val40(a[k].y) + val40(bq[k].y), val40(a[k].w) + val40(bq[k].w)); } while (0)
      u32 qpk = 0;
      lds_wait_ge(lds + L_CNT + 16, 4u);
      QPOLL(0);
      if (lane == 0) __hip_atomic_fetch_add((u32*)(lds + L_CNT + 28), 1u, __ATOMIC_RELAXED, __HIP_MEMORY_SCOPE_WORKGROUP);
      for (u32 spins = 0; !QOK(0) && spins < SPIN_CAP; ++spins) QPOLL(0);
      QDONE(0);
      *(u32*)(lds + L_QS + t * 4) = qpk;
      asm volatile("s_waitcnt lgkmcnt(0)" ::: "memory");
      if (lane == 0) __hip_atomic_fetch_add((u32*)(lds + L_CNT + 4), 1u, __ATOMIC_RELAXED, __HIP_MEMORY_SCOPE_WORKGROUP);
    }
    PIN();
    STAMP(4);
    STAMP(5);
    STAMP_FLUSH();
    return;
  }
  if (tid < 8) *(u32*)(lds + L_CNT + tid * 4) = 0u;
  const u64 E = ld_agent((const GAS u64*)ws);
  asm volatile("s_waitcnt lgkmcnt(0)" ::: "memory");
  PIN();
  __builtin_amdgcn_s_barrier();
  PIN();
  u32x4 Kf[8], Vc[8];
  const size_t kvbase = ((size_t)g * Sk + (Sk - WIN) + j * 128 + wv * 32) * DH;
  u32x4 Wo[4], W2[2];
  const __amdgpu_buffer_rsrc_t r2 = MKRS(w2_, DFF * D * 2), rwo = MKRS(wo_, NQ * D * 2);
  const int o2 = ((64 * jf + 8 * g + r) * D + 128 * wv + 8 * c) * 2, oo = (((g * 32 + j) * 16 + r) * D + 128 * wv + 8 * c) * 2;
  const __amdgpu_buffer_rsrc_t rk = MKRS(kc_ + kvbase, 32 * DH * 2), rv = MKRS(vc_ + kvbase, 32 * DH * 2);
#pragma unroll
  for (int i = 0; i < 4; ++i) Wo[i] = LDB(rwo, oo + 4 * i * D * 2, AUXS);
  W2[0] = LDB(r2, o2, AUXS); W2[1] = LDB(r2, o2 + 4 * D * 2, AUXS);
  u32 tch = 0, tch2 = 0;
  if (lane == 0) { tch = *(const GAS u32*)((const GAS u16*)kc_ + kvbase); tch2 = *(const GAS u32*)((const GAS u16*)vc_ + kvbase); }
  lds_wait_ge(lds + L_CNT + 24, 1u);
  PIN();
#pragma unroll
  for (int i = 0; i < 4; ++i) Kf[i] = LDB(rk, ((4 * i + r) * DH + 8 * c) * 2, AUXS);
  PIN();
#pragma unroll
  for (int i = 4; i < 8; ++i) Kf[i] = LDB(rk, ((4 * i + r) * DH + 8 * c) * 2, AUXS);
  PIN();
#pragma unroll
  for (int i = 0; i < 8; ++i) Vc[i] = LDB(rv, ((4 * i + r) * DH + 8 * c) * 2, AUXS);
  PIN();
  const GAS u64* hp = (const GAS u64*)(ws + WS_H) + ((jf * 8 + g) * 8 + (lane >> 3)) * 16 + (lane & 7);
  PIN();
  STAMP(1);
  const u64 tagE = E + 1;
  const u32 tlo = (u32)tagE, thi8 = (u32)(tagE >> 32) & 0xffu;
  asm volatile("" :: "v"(tch), "v"(tch2));
  asm volatile("" :: "v"(cpf0), "v"(cpf1));
#pragma unroll
  for (int i = 0; i < 8; ++i) *(u32x4*)(lds + L_KT + voff(32 * wv + 4 * i + r, c)) = Kf[i];
  PIN();
  if (lane == 0) __hip_atomic_fetch_add((u32*)(lds + L_CNT + 16), 1u, __ATOMIC_RELAXED, __HIP_MEMORY_SCOPE_WORKGROUP);
  u64 hg = ld_agent(hp);
  PIN();
#pragma unroll
  for (int i = 0; i < 8; ++i) Kf[i] = *(const u32x4*)(lds + L_KT + voff(32 * wv + (i >> 2) * 16 + c, 4 * (i & 3) + r));
  PIN();
#pragma unroll
  for (int i = 0; i < 8; ++i) *(u32x4*)(lds + L_VT + voff(32 * wv + 4 * i + r, c)) = Vc[i];
  u32x4 Vf[8];
  {
    const int q4 = c >> 2, p4 = c & 3;
#pragma unroll
    for (int n = 0; n < 8; ++n) {
      const u32x2 w0 = __builtin_bit_cast(u32x2, __builtin_amdgcn_ds_read_tr16_b64_v4i16((LAS s16x4*)(lds + L_VT + voff(32 * wv + 4 * r + q4, 2 * n + (p4 >> 1)) + 8 * (p4 & 1))));
      const u32x2 w1 = __builtin_bit_cast(u32x2, __builtin_amdgcn_ds_read_tr16_b64_v4i16((LAS s16x4*)(lds + L_VT + voff(32 * wv + 16 + 4 * r + q4, 2 * n + (p4 >> 1)) + 8 * (p4 & 1))));
      Vf[n] = u32x4{w0.x, w0.y, w1.x, w1.y};
    }
  }
  STAMP(11);
  float y[8];
  {
    if (has_ffn) {
      for (u32 spins = 0; !__all(bad40(hg, tlo, thi8) == 0) && spins < SPIN_CAP; ++spins) hg = ld_agent(hp);
    }
    float hv = has_ffn ? val40((u32)(hg >> 32)) : 0.f;
    hv += dpp<XOR8>(hv); hv = xrow_sum(hv);
    hv = fmaxf(bfr(hv), 0.f);
    const float h0 = __shfl(hv, r), h1 = __shfl(hv, r + 4);
#pragma unroll
    for (int k = 0; k < 4; ++k) {
      y[2 * k] = fmaf(h0, lo16(W2[0][k]), h1 * lo16(W2[1][k]));
      y[2 * k + 1] = fmaf(h0, hi16(W2[0][k]), h1 * hi16(W2[1][k]));
    }
  }
  PIN();
  STAMP(2);
  lds_spin_ge(lds + L_CNT + 4, 4u);
  STAMP(3);
  {
    f32x4 a0 = {0.f, 0.f, 0.f, 0.f}, a1 = {0.f, 0.f, 0.f, 0.f};
#pragma unroll
    for (int i = 0; i < 4; ++i) {
      const u32x4 qf = *(const u32x4*)(lds + L_QS + (c & 3) * 256 + 64 * i + 16 * r);
      a0 = mfma16(Kf[i], qf, a0);
      a1 = mfma16(Kf[4 + i], qf, a1);
    }
    float sc[8];
#pragma unroll
    for (int i = 0; i < 4; ++i) { sc[i] = bfr(bfr(a0[i]) * 0.08838834764831845f); sc[4 + i] = bfr(bfr(a1[i]) * 0.08838834764831845f); }
    const float mw = xrow_max(fmaxf(fmaxf(fmaxf(sc[0], sc[1]), fmaxf(sc[2], sc[3])), fmaxf(fmaxf(sc[4], sc[5]), fmaxf(sc[6], sc[7]))));
#pragma unroll
    for (int i = 0; i < 8; ++i) sc[i] = __builtin_amdgcn_exp2f((sc[i] - mw) * LOG2E);
    const u32x4 pf = {pk2(sc[0], sc[1]), pk2(sc[2], sc[3]), pk2(sc[4], sc[5]), pk2(sc[6], sc[7])};
    const u32x4 ones = {0x3f803f80u, 0x3f803f80u, 0x3f803f80u, 0x3f803f80u};
    const f32x4 z = {0.f, 0.f, 0.f, 0.f};
    float ow[8];
#pragma unroll
    for (int n = 0; n < 8; ++n) ow[n] = sel4(mfma16(pf, Vf[n], z), r);
    const f32x4 lac = mfma16(pf, ones, z);
    *(f32x4*)(lds + L_OW + ((wv * 4 + r) * 16 + c) * 32) = f32x4{ow[0], ow[1], ow[2], ow[3]};
    *(f32x4*)(lds + L_OW + ((wv * 4 + r) * 16 + c) * 32 + 16) = f32x4{ow[4], ow[5], ow[6], ow[7]};
    if (lane < 4) { *(float*)(lds + L_WML + (lane * 4 + wv) * 4) = mw; *(float*)(lds + L_WML + 64 + (lane * 4 + wv) * 4) = sel4(lac, lane); }
  }
  STAMP(4);
  __syncthreads();
  STAMP(5);
  {
    const f32x4 m4 = *(const f32x4*)(lds + L_WML + r * 16), l4 = *(const f32x4*)(lds + L_WML + 64 + r * 16);
    f32x2 pv[4];
#pragma unroll
    for (int w = 0; w < 4; ++w) pv[w] = *(const f32x2*)(lds + L_OW + ((w * 4 + r) * 16 + c) * 32 + wv * 8);
    const float M = fmaxf(fmaxf(m4.x, m4.y), fmaxf(m4.z, m4.w));
    float e[4];
#pragma unroll
    for (int w = 0; w < 4; ++w) e[w] = __builtin_amdgcn_exp2f((m4[w] - M) * LOG2E);
    const float L = fmaf(e[0], l4.x, fmaf(e[1], l4.y, fmaf(e[2], l4.z, e[3] * l4.w)));
    const float rl = __builtin_amdgcn_rcpf(L);
    const float x0 = fmaf(e[0], pv[0].x, fmaf(e[1], pv[1].x, fmaf(e[2], pv[2].x, e[3] * pv[3].x))) * rl;
    const float x1 = fmaf(e[0], pv[0].y, fmaf(e[1], pv[1].y, fmaf(e[2], pv[2].y, e[3] * pv[3].y))) * rl;
    const float y0 = dpp<XOR1>(x0), y1 = dpp<XOR1>(x1);
    const bool odd = c & 1;
    const u32 pr = odd ? pk2(y1, x1) : pk2(x0, y0);
    const u64 lg = pack40(fmaf(M, LOG2E, __builtin_amdgcn_logf(L)), tlo, thi8);
    const __amdgpu_buffer_rsrc_t ro = __builtin_amdgcn_make_buffer_rsrc((void*)(ws_ + WS_O), 0, (int)(256 * 512 * 8), 0x00020000);
    __builtin_amdgcn_raw_buffer_store_b128(u32x4{pr, tlo, (u32)lg, (u32)(lg >> 32)}, ro, ((g * 32 + r * 8 + 2 * wv + (c & 1)) * 32 + j) * 128 + (c >> 1) * 16, 0, 16);
  }
  PIN();
  STAMP(6);
  {
    const __amdgpu_buffer_rsrc_t ro = __builtin_amdgcn_make_buffer_rsrc((void*)(ws_ + WS_O), 0, (int)(256 * 512 * 8), 0x00020000);
    const int off = (g * 32 + j) * 4096 + t * 16;
    u32x4 og;
    f32x2 wf[16];
#pragma unroll
    for (int i = 0; i < 4; ++i)
#pragma unroll
      for (int k = 0; k < 4; ++k) { wf[4 * i + k] = f32x2{lo16(Wo[i][k]), hi16(Wo[i][k])}; asm volatile("" : "+v"(wf[4 * i + k])); }
    if (D2) __builtin_amdgcn_s_sleep(D2);
    for (u32 spins = 0;; ++spins) {
      asm volatile("" ::: "memory");
      og = __builtin_amdgcn_raw_buffer_load_b128(ro, off, 0, 16);
      const u32 bad = (og.y ^ tlo) | (og.z ^ tlo) | ((og.w ^ thi8) & 0xffu);
      if (__all(bad == 0) || spins >= SPIN_CAP) break;
    }
    STAMP(7);
    const float lv = val40(og.w);
    float Mw = lv;
    Mw = fmaxf(Mw, dpp<XOR8>(Mw)); Mw = xrow_max(Mw);
    const float mine = __builtin_amdgcn_exp2f(lv - Mw);
    float Ww = mine;
    Ww += dpp<XOR8>(Ww); Ww = xrow_sum(Ww);
    float v = tr32(mine * lo16(og.x), mine * hi16(og.x));
    { auto tt = __builtin_amdgcn_permlane16_swap(__float_as_uint(v), __float_as_uint(v), false, false); v = __uint_as_float(tt[0]) + __uint_as_float(tt[1]); }
    v += dpp<XOR8>(v);
    if ((lane & 24) == 0) *(float*)(lds + L_ORED + ((2 * (lane & 7) + (lane >> 5)) * 4 + wv) * 4) = v;
    if (lane == 8) { *(float*)(lds + L_HV + wv * 4) = Mw; *(float*)(lds + L_HV + 16 + wv * 4) = Ww; }
    __syncthreads();
    const f32x4 m4 = *(const f32x4*)(lds + L_HV), w4 = *(const f32x4*)(lds + L_HV + 16);
    const float Mx = fmaxf(fmaxf(m4.x, m4.y), fmaxf(m4.z, m4.w));
    f32x4 e4;
#pragma unroll
    for (int w = 0; w < 4; ++w) e4[w] = __builtin_amdgcn_exp2f(m4[w] - Mx);
    const float rL = __builtin_amdgcn_rcpf(fmaf(e4.x, w4.x, fmaf(e4.y, w4.y, fmaf(e4.z, w4.z, e4.w * w4.w))));
#pragma unroll
    for (int i = 0; i < 4; ++i) {
      const f32x4 a0 = *(const f32x4*)(lds + L_ORED + (r + 4 * i) * 16);
      const float o = bfr(fmaf(e4.x, a0.x, fmaf(e4.y, a0.y, fmaf(e4.z, a0.z, e4.w * a0.w))) * rL);
#pragma unroll
      for (int k = 0; k < 4; ++k) { y[2 * k] = fmaf(o, wf[4 * i + k].x, y[2 * k]); y[2 * k + 1] = fmaf(o, wf[4 * i + k].y, y[2 * k + 1]); }
    }
    const float a0 = tr32(y[0], y[4]), a1 = tr32(y[1], y[5]), a2 = tr32(y[2], y[6]), a3 = tr32(y[3], y[7]);
    const float b0 = tr16(a0, a2), b1 = tr16(a1, a3);
    const u64 g0 = pack40(b0, tlo, thi8), g1 = pack40(b1, tlo, thi8);
    const __amdgpu_buffer_rsrc_t rf = __builtin_amdgcn_make_buffer_rsrc((void*)(ws_ + WS_F), 0, (int)(256 * 512 * 8), 0x00020000);
    const int offf = (((16 * wv + c) * 256 + b) * CPR + (lane >> 5) * 4 + ((lane >> 4) & 1) * 2) * 8;
    __builtin_amdgcn_raw_buffer_store_b128(u32x4{(u32)g0, (u32)(g0 >> 32), (u32)g1, (u32)(g1 >> 32)}, rf, offf, 0, 16);
  }
  PIN();
  STAMP(10);
  if (b >= NRED) { STAMP_FLUSH(); return; }
  {
    const __amdgpu_buffer_rsrc_t rs = __builtin_amdgcn_make_buffer_rsrc((void*)(ws_ + WS_F), 0, (int)(256 * 512 * 8), 0x00020000);
    const int off = b * 16384 + tid * 16;
    const u32 xo = x[b * CPR + (tid & 7)];
    if (D3) __builtin_amdgcn_s_sleep(D3);
    u32x4 fg[4];
    for (u32 spins = 0;; ++spins) {
      asm volatile("" ::: "memory");
#pragma unroll
      for (int k = 0; k < 4; ++k) fg[k] = __builtin_amdgcn_raw_buffer_load_b128(rs, off + 4096 * k, 0, 16);
      u32 bad = 0;
#pragma unroll
      for (int k = 0; k < 4; ++k) bad |= (fg[k].x ^ tlo) | ((fg[k].y ^ thi8) & 0xffu) | (fg[k].z ^ tlo) | ((fg[k].w ^ thi8) & 0xffu);
      if (__all(bad == 0) || spins >= SPIN_CAP) break;
    }
    STAMP(8);
    const float s0 = (val40(fg[0].y) + val40(fg[1].y)) + (val40(fg[2].y) + val40(fg[3].y));
    const float s1 = (val40(fg[0].w) + val40(fg[1].w)) + (val40(fg[2].w) + val40(fg[3].w));
    float s = tr32(s0, s1);
    { auto tt = __builtin_amdgcn_permlane16_swap(__float_as_uint(s), __float_as_uint(s), false, false); s = __uint_as_float(tt[0]) + __uint_as_float(tt[1]); }
    s += dpp<XOR8>(s);
    s += __builtin_bit_cast(float, __builtin_amdgcn_ds_swizzle(__builtin_bit_cast(int, s), 0x101F));
    if ((lane & 28) == 0) *(float*)(lds + L_FRED + ((2 * (lane & 3) + (lane >> 5)) * 4 + wave) * 4) = s;
    __syncthreads();
    if (tid < CPR) {
      const f32x4 a = *(const f32x4*)(lds + L_FRED + tid * 16);
      ((GAS u16*)out_)[b * CPR + tid] = (u16)bfbits(lo16(xo) + ((a.x + a.y) + (a.z + a.w)));
    }
    if (b == 0 && tid == 0) st_agent((GAS u64*)ws, tagE);
    STAMP(9);
    STAMP_FLUSH();
  }
}

}

extern "C" void kernel_launch(void* const* d_in, const int* in_sizes, int n_in,
                              void* d_out, int out_size, void* d_ws, size_t ws_size,
                              hipStream_t stream) {
  const u16* x = (const u16*)d_in[0];
  const u16* kc = (const u16*)d_in[1];
  const u16* vc = (const u16*)d_in[2];
  const u16* wq = (const u16*)d_in[3];
  const u16* wo = (const u16*)d_in[4];
  const u16* w1 = (const u16*)d_in[6];
  const u16* w2 = (const u16*)d_in[7];
  const int Sk = in_sizes[1] / (HKV * DH);
  fused_decode_kernel<<<NWG, NT, 0, stream>>>(x, kc, vc, wq, wo, w1, w2, (u16*)d_out, (unsigned char*)d_ws, Sk);
}
```

```cpp
#include <hip/hip_runtime.h>
#include <hip/hip_bf16.h>
#include <stdint.h>

typedef unsigned u32;
typedef unsigned long long u64;
typedef unsigned short u16;
typedef __attribute__((ext_vector_type(4))) unsigned u32x4;
typedef __attribute__((ext_vector_type(2))) unsigned u32x2;
typedef __attribute__((ext_vector_type(4))) float f32x4;
typedef __attribute__((ext_vector_type(2))) float f32x2;
typedef __attribute__((ext_vector_type(8))) short s16x8;
typedef __attribute__((ext_vector_type(4))) short s16x4;
typedef __attribute__((ext_vector_type(2))) __bf16 bf16x2;
typedef __attribute__((ext_vector_type(8))) __bf16 bf16x8;
#define GAS __attribute__((address_space(1)))
#define LAS __attribute__((address_space(3)))

namespace {

constexpr int D = 512, HKV = 8, DH = 128, NQ = 4096, DFF = 1792, WIN = 4096;
constexpr int NWG = 256, NT = 512;
constexpr int NRED = 64, CPR = D / NRED;
constexpr size_t WS_Q = 4096;
constexpr size_t WS_H = WS_Q + 8 * 32 * 256;
constexpr size_t WS_O = WS_H + 32 * 8 * 8 * 128;
constexpr size_t WS_F = WS_O + (size_t)8 * 32 * 32 * 16 * 8;
constexpr size_t WS_END = WS_F + (size_t)256 * 512 * 8;
constexpr u32 SPIN_CAP = 1u << 16;
constexpr float LOG2E = 1.4426950408889634f;

constexpr int L_VT = 0;
constexpr int L_OW = 32768;
constexpr int L_QS = 40960;
constexpr int L_QRED = 46080;
constexpr int L_WML = 42240;
constexpr int L_ORED = 42496;
constexpr int L_FRED = 42752;
constexpr int L_HV = 42400;
constexpr int L_CNT = 42880;
constexpr int L_XN = 42912;
constexpr int L_HRED = 44960;
constexpr int L_KT = 46592;
constexpr int L_TOTAL = L_KT + 32768;

__device__ __forceinline__ float bfr(float f) { return (float)(__bf16)f; }
__device__ __forceinline__ float lo16(u32 u) { return __uint_as_float(u << 16); }
__device__ __forceinline__ float hi16(u32 u) { return __uint_as_float(u & 0xffff0000u); }
__device__ __forceinline__ u32 bfbits(float f) { return (u32)__builtin_bit_cast(u16, (__bf16)f); }
__device__ __forceinline__ u32 pk2(float a, float b) { bf16x2 v = {(__bf16)a, (__bf16)b}; return __builtin_bit_cast(u32, v); }
template <int CTRL> __device__ __forceinline__ float dpp(float x) {
  return __builtin_bit_cast(float, __builtin_amdgcn_mov_dpp(__builtin_bit_cast(int, x), CTRL, 0xf, 0xf, true));
}
constexpr int XOR1 = 0xB1, XOR2 = 0x4E, XOR7 = 0x141, XOR8 = 0x128;
__device__ __forceinline__ float xrow_max(float x) {
  auto s = __builtin_amdgcn_permlane16_swap(__float_as_uint(x), __float_as_uint(x), false, false);
  x = fmaxf(__uint_as_float(s[0]), __uint_as_float(s[1]));
  auto t = __builtin_amdgcn_permlane32_swap(__float_as_uint(x), __float_as_uint(x), false, false);
  return fmaxf(__uint_as_float(t[0]), __uint_as_float(t[1]));
}
__device__ __forceinline__ float xrow_sum(float x) {
  auto s = __builtin_amdgcn_permlane16_swap(__float_as_uint(x), __float_as_uint(x), false, false);
  x = __uint_as_float(s[0]) + __uint_as_float(s[1]);
  auto t = __builtin_amdgcn_permlane32_swap(__float_as_uint(x), __float_as_uint(x), false, false);
  return __uint_as_float(t[0]) + __uint_as_float(t[1]);
}
__device__ __forceinline__ float wave_sum(float v) {
  v += dpp<XOR1>(v); v += dpp<XOR2>(v); v += dpp<XOR7>(v); v += dpp<XOR8>(v);
  return xrow_sum(v);
}
__device__ __forceinline__ float tr32(float a, float b) {
  auto t = __builtin_amdgcn_permlane32_swap(__float_as_uint(a), __float_as_uint(b), false, false);
  return __uint_as_float(t[0]) + __uint_as_float(t[1]);
}
__device__ __forceinline__ float tr16(float a, float b) {
  auto t = __builtin_amdgcn_permlane16_swap(__float_as_uint(a), __float_as_uint(b), false, false);
  return __uint_as_float(t[0]) + __uint_as_float(t[1]);
}
template <int CTRL> __device__ __forceinline__ float trd(float a, float b, bool up) {
  const float send = up ? a : b, keep = up ? b : a;
  return keep + dpp<CTRL>(send);
}
__device__ __forceinline__ float tr8_hi(const float* v, int lane) {
  const float a0 = tr32(v[0], v[4]), a1 = tr32(v[1], v[5]), a2 = tr32(v[2], v[6]), a3 = tr32(v[3], v[7]);
  const float b0 = tr16(a0, a2), b1 = tr16(a1, a3);
  return trd<XOR8>(b0, b1, lane & 8);
}
__device__ __forceinline__ float sel4(f32x4 a, int i) {
  const float lo = (i & 1) ? a[1] : a[0], hi = (i & 1) ? a[3] : a[2];
  return (i & 2) ? hi : lo;
}
__device__ __forceinline__ f32x4 mfma16(u32x4 a, u32x4 b, f32x4 c) {
  return __builtin_amdgcn_mfma_f32_16x16x32_bf16(__builtin_bit_cast(bf16x8, a), __builtin_bit_cast(bf16x8, b), c, 0, 0, 0);
}
__device__ __forceinline__ u64 ld_agent(const GAS u64* p) { return __hip_atomic_load(p, __ATOMIC_RELAXED, __HIP_MEMORY_SCOPE_AGENT); }
__device__ __forceinline__ void st_agent(GAS u64* p, u64 v) { __hip_atomic_store(p, v, __ATOMIC_RELAXED, __HIP_MEMORY_SCOPE_AGENT); }
__device__ __forceinline__ u64 pack40(float v, u32 tlo, u32 thi8) {
  const u32 b = __float_as_uint(v);
  const u32 rr = b + 0x7fu + ((b >> 8) & 1u), rn = b | 0x00400000u;
  const u32 m = (v != v) ? 0xffffffffu : 0u;
  u32 r = (rn & m) | (rr & ~m);
  r = (r & 0xffffff00u) | thi8;
  return ((u64)r << 32) | tlo;
}
__device__ __forceinline__ u32 bad40(u64 g, u32 tlo, u32 thi8) { return ((u32)g ^ tlo) | (((u32)(g >> 32) ^ thi8) & 0xffu); }
__device__ __forceinline__ float val40(u32 hi) { return __uint_as_float(hi & 0xffffff00u); }
__device__ __forceinline__ u32 voff(u32 row, u32 ch) { return 256u * row + 16u * (ch ^ (((row & 3u) << 2) | ((row >> 2) & 3u))); }
#define PIN() do { asm volatile("" ::: "memory"); __builtin_amdgcn_sched_barrier(0); } while (0)

constexpr int AUX_STREAM = 18;
constexpr int POLL_DELAY = 30;
constexpr u64 KV_DEADLINE = 140;
#define MKRS(p, n) __builtin_amdgcn_make_buffer_rsrc((void*)(p), 0, (int)(n), 0x00020000)
#define LDB(rs, off, aux) __builtin_amdgcn_raw_buffer_load_b128(rs, (int)(off), 0, aux)
__device__ __forceinline__ u32 lds_ld(const unsigned char* p) { return __hip_atomic_load((const u32*)p, __ATOMIC_RELAXED, __HIP_MEMORY_SCOPE_WORKGROUP); }
__device__ __forceinline__ void lds_spin_ge(const unsigned char* p, u32 want) {
  for (u32 spins = 0; (u32)__builtin_amdgcn_readfirstlane(lds_ld(p)) < want && spins < (1u << 24); ++spins) { }
  asm volatile("" ::: "memory");
}
__device__ __forceinline__ void lds_wait_ge(const unsigned char* p, u32 want) {
  for (u32 spins = 0; (u32)__builtin_amdgcn_readfirstlane(lds_ld(p)) < want && spins < (1u << 22); ++spins) __builtin_amdgcn_s_sleep(1);
  asm volatile("" ::: "memory");
}
__global__ void __launch_bounds__(NT, 2)
fused_decode_kernel(const u16* x_, const u16* kc_, const u16* vc_, const u16* wq_, const u16* wo_,
                    const u16* w1_, const u16* w2_, u16* out_, unsigned char* ws_, int Sk) {
  __shared__ __attribute__((aligned(16))) unsigned char lds[L_TOTAL];
  const GAS u16* x = (const GAS u16*)x_;
  const int tid = threadIdx.x, lane = tid & 63, wave = __builtin_amdgcn_readfirstlane(tid >> 6);
  const int b = blockIdx.x, g = b & 7, j = b >> 3;
  const int r = lane >> 4, c = lane & 15;
  GAS unsigned char* ws = (GAS unsigned char*)ws_;
  const bool has_ffn = j < 28;
  const int jf = has_ffn ? j : 0;
  const int wv = wave & 3, t = tid & 255;
  u32 cpf0 = 0;
  if (wave == 0) {
    const GAS u32* cp = (const GAS u32*)(__builtin_amdgcn_s_getpc() & ~127ull);
    cpf0 = cp[lane * 32];
  }
  if (__builtin_expect(wave >= 4, 1)) {
    u32x4 R[6];
    const u32x4 xv = *(const GAS u32x4*)(x + 8 * lane);
    const u64 E = ld_agent((const GAS u64*)ws);
    const __amdgpu_buffer_rsrc_t rq = MKRS(wq_, D * NQ * 2), r1 = MKRS(w1_, D * DFF * 2);
    const int qoff = ((256 * (j & 1) + (t >> 2)) * NQ + g * 512 + (j >> 1) * 32 + (t & 3) * 8) * 2;
#pragma unroll
    for (int i = 0; i < 4; ++i) R[i] = LDB(rq, qoff + 64 * i * NQ * 2, AUX_STREAM);
    const int o1 = ((64 * g + (t >> 3)) * DFF + 64 * jf + 8 * (t & 7)) * 2;
    R[4] = LDB(r1, o1, AUX_STREAM); R[5] = LDB(r1, o1 + 32 * DFF * 2, AUX_STREAM);
    PIN();
    __builtin_amdgcn_s_barrier();
    PIN();
    float xf[8];
#pragma unroll
    for (int k = 0; k < 4; ++k) { xf[2 * k] = lo16(xv[k]); xf[2 * k + 1] = hi16(xv[k]); }
    float s = 0.f;
#pragma unroll
    for (int k = 0; k < 8; ++k) s += xf[k];
    const float mean = wave_sum(s) * (1.f / D);
    float ss = 0.f;
#pragma unroll
    for (int k = 0; k < 8; ++k) { const float d = xf[k] - mean; ss = fmaf(d, d, ss); }
    const float var = wave_sum(ss) * (1.f / D);
    const float mb = bfr(mean);
    const float rden = __builtin_amdgcn_rcpf(bfr(__builtin_amdgcn_sqrtf(bfr(bfr(var) + 1e-5f))));
#pragma unroll
    for (int k = 0; k < 8; ++k) xf[k] = bfr(bfr(xf[k] - mb) * rden);
    *(f32x4*)(lds + L_XN + lane * 32) = f32x4{xf[0], xf[1], xf[2], xf[3]};
    *(f32x4*)(lds + L_XN + lane * 32 + 16) = f32x4{xf[4], xf[5], xf[6], xf[7]};
    const u64 tagE = E + 1;
    const u32 tlo = (u32)tagE, thi8 = (u32)(tagE >> 32) & 0xffu;
    {
      float v[8];
#pragma unroll
      for (int k = 0; k < 8; ++k) v[k] = 0.f;
#pragma unroll
      for (int i = 0; i < 4; ++i) {
        const float xn = *(const float*)(lds + L_XN + (256 * (j & 1) + (t >> 2) + 64 * i) * 4);
#pragma unroll
        for (int k = 0; k < 4; ++k) { v[2 * k] = fmaf(xn, lo16(R[i][k]), v[2 * k]); v[2 * k + 1] = fmaf(xn, hi16(R[i][k]), v[2 * k + 1]); }
      }
      float q = tr8_hi(v, lane);
      q += __builtin_bit_cast(float, __builtin_amdgcn_ds_swizzle(__builtin_bit_cast(int, q), 0x101F));
      if ((lane & 4) == 0) {
        const int col = (lane & 3) * 8 + (lane >> 5) * 4 + ((lane >> 4) & 1) * 2 + ((lane >> 3) & 1);
        *(float*)(lds + L_QRED + (col * 4 + wv) * 4) = q;
      }
    }
    {
      const float xa = *(const float*)(lds + L_XN + (64 * g + (t >> 3)) * 4), xb = *(const float*)(lds + L_XN + (64 * g + 32 + (t >> 3)) * 4);
      float v[8];
#pragma unroll
      for (int k = 0; k < 4; ++k) { v[2 * k] = fmaf(xa, lo16(R[4][k]), xb * lo16(R[5][k])); v[2 * k + 1] = fmaf(xa, hi16(R[4][k]), xb * hi16(R[5][k])); }
      const float h = tr8_hi(v, lane);
      *(float*)(lds + L_HRED + ((8 * (lane & 7) + (lane >> 5) * 4 + ((lane >> 4) & 1) * 2 + ((lane >> 3) & 1)) * 4 + wv) * 4) = h;
    }
    asm volatile("s_waitcnt lgkmcnt(0)" ::: "memory");
    u32 old = 0;
    if (lane == 0) old = __hip_atomic_fetch_add((u32*)(lds + L_CNT), 1u, __ATOMIC_RELAXED, __HIP_MEMORY_SCOPE_WORKGROUP);
    old = __builtin_amdgcn_readfirstlane(old);
    if (old == 3u) {
      asm volatile("" ::: "memory");
      const f32x4 a = *(const f32x4*)(lds + L_QRED + (lane & 31) * 16);
      if (lane < 32) st_agent((GAS u64*)(ws + WS_Q) + (g * 32 + j) * 32 + lane, pack40((a.x + a.y) + (a.z + a.w), tlo, thi8));
      const f32x4 hh = *(const f32x4*)(lds + L_HRED + lane * 16);
      if (has_ffn) st_agent((GAS u64*)(ws + WS_H) + ((j * 8 + (lane >> 3)) * 8 + g) * 16 + (lane & 7), pack40((hh.x + hh.y) + (hh.z + hh.w), tlo, thi8));
      PIN();
      if (lane == 0) __hip_atomic_store((u32*)(lds + L_CNT + 24), 1u, __ATOMIC_RELAXED, __HIP_MEMORY_SCOPE_WORKGROUP);
    }
    PIN();
    lds_wait_ge(lds + L_CNT + 24, 1u);
    {
      const __amdgpu_buffer_rsrc_t rs = __builtin_amdgcn_make_buffer_rsrc((void*)(ws_ + WS_Q), 0, 8 * 32 * 256, 0x00020000);
      const int off = (g * 32 + (t >> 4) * 2) * 256 + (t & 15) * 16;
      u32x4 a[1], bq[1];
#define QPOLL(k) do { asm volatile("" ::: "memory"); a[k] = __builtin_amdgcn_raw_buffer_load_b128(rs, off, 0, 16); bq[k] = __builtin_amdgcn_raw_buffer_load_b128(rs, off + 256, 0, 16); PIN(); } while (0)
#define QBAD(v) ((v.x ^ tlo) | ((v.y ^ thi8) & 0xffu) | (v.z ^ tlo) | ((v.w ^ thi8) & 0xffu))
#define QOK(k) __all((QBAD(a[k]) | QBAD(bq[k])) == 0)
#define QDONE(k) do { qpk = pk2(val40(a[k].y) + val40(bq[k].y), val40(a[k].w) + val40(bq[k].w)); } while (0)
      u32 qpk = 0;
      lds_wait_ge(lds + L_CNT + 16, 4u);
      QPOLL(0);
      for (u32 spins = 0; !QOK(0) && spins < SPIN_CAP; ++spins) QPOLL(0);
      QDONE(0);
      *(u32*)(lds + L_QS + t * 4) = qpk;
      asm volatile("s_waitcnt lgkmcnt(0)" ::: "memory");
      if (lane == 0) __hip_atomic_fetch_add((u32*)(lds + L_CNT + 4), 1u, __ATOMIC_RELAXED, __HIP_MEMORY_SCOPE_WORKGROUP);
    }
    PIN();
    return;
  }
  if (tid < 8) *(u32*)(lds + L_CNT + tid * 4) = 0u;
  const u64 E = ld_agent((const GAS u64*)ws);
  asm volatile("s_waitcnt lgkmcnt(0)" ::: "memory");
  PIN();
  __builtin_amdgcn_s_barrier();
  PIN();
  u32x4 Kf[8], Vc[8];
  const size_t kvbase = ((size_t)g * Sk + (Sk - WIN) + j * 128 + wv * 32) * DH;
  u32x4 Wo[4], W2[2];
  const __amdgpu_buffer_rsrc_t r2 = MKRS(w2_, DFF * D * 2), rwo = MKRS(wo_, NQ * D * 2);
  const int o2 = ((64 * jf + 8 * g + r) * D + 128 * wv + 8 * c) * 2, oo = (((g * 32 + j) * 16 + r) * D + 128 * wv + 8 * c) * 2;
  const __amdgpu_buffer_rsrc_t rk = MKRS(kc_ + kvbase, 32 * DH * 2), rv = MKRS(vc_ + kvbase, 32 * DH * 2);
#pragma unroll
  for (int i = 0; i < 4; ++i) Wo[i] = LDB(rwo, oo + 4 * i * D * 2, AUX_STREAM);
  W2[0] = LDB(r2, o2, AUX_STREAM); W2[1] = LDB(r2, o2 + 4 * D * 2, AUX_STREAM);
  u32 tch = 0, tch2 = 0, tch3 = 0;
  if (lane == 0) {
    tch = *(const GAS u32*)((const GAS u16*)kc_ + kvbase); tch2 = *(const GAS u32*)((const GAS u16*)vc_ + kvbase);
    tch3 = *(const GAS u32*)(wv == 0 ? (const GAS unsigned char*)out_ : ws + WS_END + 64);
  }
  {
    const u64 t0 = __builtin_amdgcn_s_memrealtime();
    while ((u32)__builtin_amdgcn_readfirstlane(lds_ld(lds + L_CNT + 24)) < 1u && __builtin_amdgcn_s_memrealtime() - t0 < KV_DEADLINE) __builtin_amdgcn_s_sleep(1);
    asm volatile("" ::: "memory");
  }
  PIN();
#pragma unroll
  for (int i = 0; i < 4; ++i) Kf[i] = LDB(rk, ((4 * i + r) * DH + 8 * c) * 2, AUX_STREAM);
  PIN();
#pragma unroll
  for (int i = 4; i < 8; ++i) Kf[i] = LDB(rk, ((4 * i + r) * DH + 8 * c) * 2, AUX_STREAM);
  PIN();
#pragma unroll
  for (int i = 0; i < 8; ++i) Vc[i] = LDB(rv, ((4 * i + r) * DH + 8 * c) * 2, AUX_STREAM);
  PIN();
  const GAS u64* hp = (const GAS u64*)(ws + WS_H) + ((jf * 8 + g) * 8 + (lane >> 3)) * 16 + (lane & 7);
  PIN();
  const u64 tagE = E + 1;
  const u32 tlo = (u32)tagE, thi8 = (u32)(tagE >> 32) & 0xffu;
  asm volatile("" :: "v"(tch), "v"(tch2), "v"(tch3));
  asm volatile("" :: "v"(cpf0));
#pragma unroll
  for (int i = 0; i < 8; ++i) *(u32x4*)(lds + L_KT + voff(32 * wv + 4 * i + r, c)) = Kf[i];
  PIN();
  if (lane == 0) __hip_atomic_fetch_add((u32*)(lds + L_CNT + 16), 1u, __ATOMIC_RELAXED, __HIP_MEMORY_SCOPE_WORKGROUP);
  u64 hg = ld_agent(hp);
  PIN();
#pragma unroll
  for (int i = 0; i < 8; ++i) Kf[i] = *(const u32x4*)(lds + L_KT + voff(32 * wv + (i >> 2) * 16 + c, 4 * (i & 3) + r));
  PIN();
#pragma unroll
  for (int i = 0; i < 8; ++i) *(u32x4*)(lds + L_VT + voff(32 * wv + 4 * i + r, c)) = Vc[i];
  u32x4 Vf[8];
  {
    const int q4 = c >> 2, p4 = c & 3;
#pragma unroll
    for (int n = 0; n < 8; ++n) {
      const u32x2 w0 = __builtin_bit_cast(u32x2, __builtin_amdgcn_ds_read_tr16_b64_v4i16((LAS s16x4*)(lds + L_VT + voff(32 * wv + 4 * r + q4, 2 * n + (p4 >> 1)) + 8 * (p4 & 1))));
      const u32x2 w1 = __builtin_bit_cast(u32x2, __builtin_amdgcn_ds_read_tr16_b64_v4i16((LAS s16x4*)(lds + L_VT + voff(32 * wv + 16 + 4 * r + q4, 2 * n + (p4 >> 1)) + 8 * (p4 & 1))));
      Vf[n] = u32x4{w0.x, w0.y, w1.x, w1.y};
    }
  }
  float y[8];
  {
    if (has_ffn) {
      for (u32 spins = 0; !__all(bad40(hg, tlo, thi8) == 0) && spins < SPIN_CAP; ++spins) hg = ld_agent(hp);
    }
    float hv = has_ffn ? val40((u32)(hg >> 32)) : 0.f;
    hv += dpp<XOR8>(hv); hv = xrow_sum(hv);
    hv = fmaxf(bfr(hv), 0.f);
    const float h0 = __shfl(hv, r), h1 = __shfl(hv, r + 4);
#pragma unroll
    for (int k = 0; k < 4; ++k) {
      y[2 * k] = fmaf(h0, lo16(W2[0][k]), h1 * lo16(W2[1][k]));
      y[2 * k + 1] = fmaf(h0, hi16(W2[0][k]), h1 * hi16(W2[1][k]));
    }
  }
  PIN();
  lds_spin_ge(lds + L_CNT + 4, 4u);
  {
    f32x4 a0 = {0.f, 0.f, 0.f, 0.f}, a1 = {0.f, 0.f, 0.f, 0.f};
#pragma unroll
    for (int i = 0; i < 4; ++i) {
      const u32x4 qf = *(const u32x4*)(lds + L_QS + (c & 3) * 256 + 64 * i + 16 * r);
      a0 = mfma16(Kf[i], qf, a0);
      a1 = mfma16(Kf[4 + i], qf, a1);
    }
    float sc[8];
#pragma unroll
    for (int i = 0; i < 4; ++i) { sc[i] = bfr(bfr(a0[i]) * 0.08838834764831845f); sc[4 + i] = bfr(bfr(a1[i]) * 0.08838834764831845f); }
    const float mw = xrow_max(fmaxf(fmaxf(fmaxf(sc[0], sc[1]), fmaxf(sc[2], sc[3])), fmaxf(fmaxf(sc[4], sc[5]), fmaxf(sc[6], sc[7]))));
#pragma unroll
    for (int i = 0; i < 8; ++i) sc[i] = __builtin_amdgcn_exp2f((sc[i] - mw) * LOG2E);
    const u32x4 pf = {pk2(sc[0], sc[1]), pk2(sc[2], sc[3]), pk2(sc[4], sc[5]), pk2(sc[6], sc[7])};
    const u32x4 ones = {0x3f803f80u, 0x3f803f80u, 0x3f803f80u, 0x3f803f80u};
    const f32x4 z = {0.f, 0.f, 0.f, 0.f};
    float ow[8];
#pragma unroll
    for (int n = 0; n < 8; ++n) ow[n] = sel4(mfma16(pf, Vf[n], z), r);
    const f32x4 lac = mfma16(pf, ones, z);
    *(f32x4*)(lds + L_OW + ((wv * 4 + r) * 16 + c) * 32) = f32x4{ow[0], ow[1], ow[2], ow[3]};
    *(f32x4*)(lds + L_OW + ((wv * 4 + r) * 16 + c) * 32 + 16) = f32x4{ow[4], ow[5], ow[6], ow[7]};
    if (lane < 4) { *(float*)(lds + L_WML + (lane * 4 + wv) * 4) = mw; *(float*)(lds + L_WML + 64 + (lane * 4 + wv) * 4) = sel4(lac, lane); }
  }
  __syncthreads();
  {
    const f32x4 m4 = *(const f32x4*)(lds + L_WML + r * 16), l4 = *(const f32x4*)(lds + L_WML + 64 + r * 16);
    f32x2 pv[4];
#pragma unroll
    for (int w = 0; w < 4; ++w) pv[w] = *(const f32x2*)(lds + L_OW + ((w * 4 + r) * 16 + c) * 32 + wv * 8);
    const float M = fmaxf(fmaxf(m4.x, m4.y), fmaxf(m4.z, m4.w));
    float e[4];
#pragma unroll
    for (int w = 0; w < 4; ++w) e[w] = __builtin_amdgcn_exp2f((m4[w] - M) * LOG2E);
    const float L = fmaf(e[0], l4.x, fmaf(e[1], l4.y, fmaf(e[2], l4.z, e[3] * l4.w)));
    const float rl = __builtin_amdgcn_rcpf(L);
    const float x0 = fmaf(e[0], pv[0].x, fmaf(e[1], pv[1].x, fmaf(e[2], pv[2].x, e[3] * pv[3].x))) * rl;
    const float x1 = fmaf(e[0], pv[0].y, fmaf(e[1], pv[1].y, fmaf(e[2], pv[2].y, e[3] * pv[3].y))) * rl;
    const float y0 = dpp<XOR1>(x0), y1 = dpp<XOR1>(x1);
    const bool odd = c & 1;
    const u32 pr = odd ? pk2(y1, x1) : pk2(x0, y0);
    const u64 lg = pack40(fmaf(M, LOG2E, __builtin_amdgcn_logf(L)), tlo, thi8);
    const __amdgpu_buffer_rsrc_t ro = __builtin_amdgcn_make_buffer_rsrc((void*)(ws_ + WS_O), 0, (int)(256 * 512 * 8), 0x00020000);
    __builtin_amdgcn_raw_buffer_store_b128(u32x4{pr, tlo, (u32)lg, (u32)(lg >> 32)}, ro, ((g * 32 + r * 8 + 2 * wv + (c & 1)) * 32 + j) * 128 + (c >> 1) * 16, 0, 16);
  }
  PIN();
  {
    const __amdgpu_buffer_rsrc_t ro = __builtin_amdgcn_make_buffer_rsrc((void*)(ws_ + WS_O), 0, (int)(256 * 512 * 8), 0x00020000);
    const int off = (g * 32 + j) * 4096 + t * 16;
    u32x4 og;
    f32x2 wf[16];
#pragma unroll
    for (int i = 0; i < 4; ++i)
#pragma unroll
      for (int k = 0; k < 4; ++k) { wf[4 * i + k] = f32x2{lo16(Wo[i][k]), hi16(Wo[i][k])}; asm volatile("" : "+v"(wf[4 * i + k])); }
    __builtin_amdgcn_s_sleep(POLL_DELAY);
    for (u32 spins = 0;; ++spins) {
      asm volatile("" ::: "memory");
      og = __builtin_amdgcn_raw_buffer_load_b128(ro, off, 0, 16);
      const u32 bad = (og.y ^ tlo) | (og.z ^ tlo) | ((og.w ^ thi8) & 0xffu);
      if (__all(bad == 0) || spins >= SPIN_CAP) break;
    }
    const float lv = val40(og.w);
    float Mw = lv;
    Mw = fmaxf(Mw, dpp<XOR8>(Mw)); Mw = xrow_max(Mw);
    const float mine = __builtin_amdgcn_exp2f(lv - Mw);
    float Ww = mine;
    Ww += dpp<XOR8>(Ww); Ww = xrow_sum(Ww);
    float v = tr32(mine * lo16(og.x), mine * hi16(og.x));
    { auto tt = __builtin_amdgcn_permlane16_swap(__float_as_uint(v), __float_as_uint(v), false, false); v = __uint_as_float(tt[0]) + __uint_as_float(tt[1]); }
    v += dpp<XOR8>(v);
    if ((lane & 24) == 0) *(float*)(lds + L_ORED + ((2 * (lane & 7) + (lane >> 5)) * 4 + wv) * 4) = v;
    if (lane == 8) { *(float*)(lds + L_HV + wv * 4) = Mw; *(float*)(lds + L_HV + 16 + wv * 4) = Ww; }
    __syncthreads();
    const f32x4 m4 = *(const f32x4*)(lds + L_HV), w4 = *(const f32x4*)(lds + L_HV + 16);
    const float Mx = fmaxf(fmaxf(m4.x, m4.y), fmaxf(m4.z, m4.w));
    f32x4 e4;
#pragma unroll
    for (int w = 0; w < 4; ++w) e4[w] = __builtin_amdgcn_exp2f(m4[w] - Mx);
    const float rL = __builtin_amdgcn_rcpf(fmaf(e4.x, w4.x, fmaf(e4.y, w4.y, fmaf(e4.z, w4.z, e4.w * w4.w))));
#pragma unroll
    for (int i = 0; i < 4; ++i) {
      const f32x4 a0 = *(const f32x4*)(lds + L_ORED + (r + 4 * i) * 16);
      const float o = bfr(fmaf(e4.x, a0.x, fmaf(e4.y, a0.y, fmaf(e4.z, a0.z, e4.w * a0.w))) * rL);
#pragma unroll
      for (int k = 0; k < 4; ++k) { y[2 * k] = fmaf(o, wf[4 * i + k].x, y[2 * k]); y[2 * k + 1] = fmaf(o, wf[4 * i + k].y, y[2 * k + 1]); }
    }
    const float a0 = tr32(y[0], y[4]), a1 = tr32(y[1], y[5]), a2 = tr32(y[2], y[6]), a3 = tr32(y[3], y[7]);
    const float b0 = tr16(a0, a2), b1 = tr16(a1, a3);
    const u64 g0 = pack40(b0, tlo, thi8), g1 = pack40(b1, tlo, thi8);
    const __amdgpu_buffer_rsrc_t rf = __builtin_amdgcn_make_buffer_rsrc((void*)(ws_ + WS_F), 0, (int)(256 * 512 * 8), 0x00020000);
    const int offf = (((16 * wv + c) * 256 + b) * CPR + (lane >> 5) * 4 + ((lane >> 4) & 1) * 2) * 8;
    __builtin_amdgcn_raw_buffer_store_b128(u32x4{(u32)g0, (u32)(g0 >> 32), (u32)g1, (u32)(g1 >> 32)}, rf, offf, 0, 16);
  }
  PIN();
  if (b >= NRED) return;
  {
    const __amdgpu_buffer_rsrc_t rs = __builtin_amdgcn_make_buffer_rsrc((void*)(ws_ + WS_F), 0, (int)(256 * 512 * 8), 0x00020000);
    const int off = b * 16384 + tid * 16;
    const u32 xo = x[b * CPR + (tid & 7)];
    __builtin_amdgcn_s_sleep(POLL_DELAY);
    u32x4 fg[4];
    for (u32 spins = 0;; ++spins) {
      asm volatile("" ::: "memory");
#pragma unroll
      for (int k = 0; k < 4; ++k) fg[k] = __builtin_amdgcn_raw_buffer_load_b128(rs, off + 4096 * k, 0, 16);
      u32 bad = 0;
#pragma unroll
      for (int k = 0; k < 4; ++k) bad |= (fg[k].x ^ tlo) | ((fg[k].y ^ thi8) & 0xffu) | (fg[k].z ^ tlo) | ((fg[k].w ^ thi8) & 0xffu);
      if (__all(bad == 0) || spins >= SPIN_CAP) break;
    }
    const float s0 = (val40(fg[0].y) + val40(fg[1].y)) + (val40(fg[2].y) + val40(fg[3].y));
    const float s1 = (val40(fg[0].w) + val40(fg[1].w)) + (val40(fg[2].w) + val40(fg[3].w));
    float s = tr32(s0, s1);
    { auto tt = __builtin_amdgcn_permlane16_swap(__float_as_uint(s), __float_as_uint(s), false, false); s = __uint_as_float(tt[0]) + __uint_as_float(tt[1]); }
    s += dpp<XOR8>(s);
    s += __builtin_bit_cast(float, __builtin_amdgcn_ds_swizzle(__builtin_bit_cast(int, s), 0x101F));
    if ((lane & 28) == 0) *(float*)(lds + L_FRED + ((2 * (lane & 3) + (lane >> 5)) * 4 + wave) * 4) = s;
    __syncthreads();
    if (tid < CPR) {
      const f32x4 a = *(const f32x4*)(lds + L_FRED + tid * 16);
      ((GAS u16*)out_)[b * CPR + tid] = (u16)bfbits(lo16(xo) + ((a.x + a.y) + (a.z + a.w)));
    }
    if (b == 0 && tid == 0) st_agent((GAS u64*)ws, tagE);
  }
}

}

extern "C" void kernel_launch(void* const* d_in, const int* in_sizes, int n_in,
                              void* d_out, int out_size, void* d_ws, size_t ws_size,
                              hipStream_t stream) {
  const u16* x = (const u16*)d_in[0];
  const u16* kc = (const u16*)d_in[1];
  const u16* vc = (const u16*)d_in[2];
  const u16* wq = (const u16*)d_in[3];
  const u16* wo = (const u16*)d_in[4];
  const u16* w1 = (const u16*)d_in[6];
  const u16* w2 = (const u16*)d_in[7];
  const int Sk = in_sizes[1] / (HKV * DH);
  fused_decode_kernel<<<NWG, NT, 0, stream>>>(x, kc, vc, wq, wo, w1, w2, (u16*)d_out, (unsigned char*)d_ws, Sk);
}
```

```cpp
#include <hip/hip_runtime.h>
#include <hip/hip_bf16.h>
#include <stdint.h>

typedef unsigned u32;
typedef unsigned long long u64;
typedef unsigned short u16;
typedef __attribute__((ext_vector_type(4))) unsigned u32x4;
typedef __attribute__((ext_vector_type(2))) unsigned u32x2;
typedef __attribute__((ext_vector_type(4))) float f32x4;
typedef __attribute__((ext_vector_type(2))) float f32x2;
typedef __attribute__((ext_vector_type(8))) short s16x8;
typedef __attribute__((ext_vector_type(4))) short s16x4;
typedef __attribute__((ext_vector_type(2))) __bf16 bf16x2;
typedef __attribute__((ext_vector_type(8))) __bf16 bf16x8;
#define GAS __attribute__((address_space(1)))
#define LAS __attribute__((address_space(3)))

namespace {

constexpr int D = 512, HKV = 8, DH = 128, NQ = 4096, DFF = 1792, WIN = 4096;
constexpr int NWG = 256, NT = 512;
constexpr int NRED = 64, CPR = D / NRED;
constexpr size_t WS_Q = 4096;
constexpr size_t WS_H = WS_Q + 8 * 32 * 256;
constexpr size_t WS_O = WS_H + 32 * 8 * 8 * 128;
constexpr size_t WS_F = WS_O + (size_t)8 * 32 * 32 * 16 * 8;
constexpr size_t WS_END = WS_F + (size_t)256 * 512 * 8;
constexpr u32 SPIN_CAP = 1u << 16;
constexpr float LOG2E = 1.4426950408889634f;

constexpr int L_VT = 0;
constexpr int L_OW = 32768;
constexpr int L_QS = 40960;
constexpr int L_QRED = 46080;
constexpr int L_WML = 42240;
constexpr int L_ORED = 42496;
constexpr int L_FRED = 42752;
constexpr int L_HV = 42400;
constexpr int L_CNT = 42880;
constexpr int L_XN = 42912;
constexpr int L_HRED = 44960;
constexpr int L_KT = 46592;
constexpr int L_TOTAL = L_KT + 32768;

__device__ __forceinline__ float bfr(float f) { return (float)(__bf16)f; }
__device__ __forceinline__ float lo16(u32 u) { return __uint_as_float(u << 16); }
__device__ __forceinline__ float hi16(u32 u) { return __uint_as_float(u & 0xffff0000u); }
__device__ __forceinline__ u32 bfbits(float f) { return (u32)__builtin_bit_cast(u16, (__bf16)f); }
__device__ __forceinline__ u32 pk2(float a, float b) { bf16x2 v = {(__bf16)a, (__bf16)b}; return __builtin_bit_cast(u32, v); }
template <int CTRL> __device__ __forceinline__ float dpp(float x) {
  return __builtin_bit_cast(float, __builtin_amdgcn_mov_dpp(__builtin_bit_cast(int, x), CTRL, 0xf, 0xf, true));
}
constexpr int XOR1 = 0xB1, XOR2 = 0x4E, XOR7 = 0x141, XOR8 = 0x128;
__device__ __forceinline__ float xrow_max(float x) {
  auto s = __builtin_amdgcn_permlane16_swap(__float_as_uint(x), __float_as_uint(x), false, false);
  x = fmaxf(__uint_as_float(s[0]), __uint_as_float(s[1]));
  auto t = __builtin_amdgcn_permlane32_swap(__float_as_uint(x), __float_as_uint(x), false, false);
  return fmaxf(__uint_as_float(t[0]), __uint_as_float(t[1]));
}
__device__ __forceinline__ float xrow_sum(float x) {
  auto s = __builtin_amdgcn_permlane16_swap(__float_as_uint(x), __float_as_uint(x), false, false);
  x = __uint_as_float(s[0]) + __uint_as_float(s[1]);
  auto t = __builtin_amdgcn_permlane32_swap(__float_as_uint(x), __float_as_uint(x), false, false);
  return __uint_as_float(t[0]) + __uint_as_float(t[1]);
}
__device__ __forceinline__ float wave_sum(float v) {
  v += dpp<XOR1>(v); v += dpp<XOR2>(v); v += dpp<XOR7>(v); v += dpp<XOR8>(v);
  return xrow_sum(v);
}
__device__ __forceinline__ float tr32(float a, float b) {
  auto t = __builtin_amdgcn_permlane32_swap(__float_as_uint(a), __float_as_uint(b), false, false);
  return __uint_as_float(t[0]) + __uint_as_float(t[1]);
}
__device__ __forceinline__ float tr16(float a, float b) {
  auto t = __builtin_amdgcn_permlane16_swap(__float_as_uint(a), __float_as_uint(b), false, false);
  return __uint_as_float(t[0]) + __uint_as_float(t[1]);
}
template <int CTRL> __device__ __forceinline__ float trd(float a, float b, bool up) {
  const float send = up ? a : b, keep = up ? b : a;
  return keep + dpp<CTRL>(send);
}
__device__ __forceinline__ float tr8_hi(const float* v, int lane) {
  const float a0 = tr32(v[0], v[4]), a1 = tr32(v[1], v[5]), a2 = tr32(v[2], v[6]), a3 = tr32(v[3], v[7]);
  const float b0 = tr16(a0, a2), b1 = tr16(a1, a3);
  return trd<XOR8>(b0, b1, lane & 8);
}
__device__ __forceinline__ float sel4(f32x4 a, int i) {
  const float lo = (i & 1) ? a[1] : a[0], hi = (i & 1) ? a[3] : a[2];
  return (i & 2) ? hi : lo;
}
__device__ __forceinline__ f32x4 mfma16(u32x4 a, u32x4 b, f32x4 c) {
  return __builtin_amdgcn_mfma_f32_16x16x32_bf16(__builtin_bit_cast(bf16x8, a), __builtin_bit_cast(bf16x8, b), c, 0, 0, 0);
}
__device__ __forceinline__ u64 ld_agent(const GAS u64* p) { return __hip_atomic_load(p, __ATOMIC_RELAXED, __HIP_MEMORY_SCOPE_AGENT); }
__device__ __forceinline__ void st_agent(GAS u64* p, u64 v) { __hip_atomic_store(p, v, __ATOMIC_RELAXED, __HIP_MEMORY_SCOPE_AGENT); }
__device__ __forceinline__ u64 pack40(float v, u32 tlo, u32 thi8) {
  const u32 b = __float_as_uint(v);
  const u32 rr = b + 0x7fu + ((b >> 8) & 1u), rn = b | 0x00400000u;
  const u32 m = (v != v) ? 0xffffffffu : 0u;
  u32 r = (rn & m) | (rr & ~m);
  r = (r & 0xffffff00u) | thi8;
  return ((u64)r << 32) | tlo;
}
__device__ __forceinline__ u32 bad40(u64 g, u32 tlo, u32 thi8) { return ((u32)g ^ tlo) | (((u32)(g >> 32) ^ thi8) & 0xffu); }
__device__ __forceinline__ float val40(u32 hi) { return __uint_as_float(hi & 0xffffff00u); }
__device__ __forceinline__ u32 voff(u32 row, u32 ch) { return 256u * row + 16u * (ch ^ (((row & 3u) << 2) | ((row >> 2) & 3u))); }
#define PIN() do { asm volatile("" ::: "memory"); __builtin_amdgcn_sched_barrier(0); } while (0)

constexpr int AUX_STREAM = 18;
constexpr int POLL_DELAY = 30;
constexpr u64 KV_DEADLINE = 100000;
#define MKRS(p, n) __builtin_amdgcn_make_buffer_rsrc((void*)(p), 0, (int)(n), 0x00020000)
#define LDB(rs, off, aux) __builtin_amdgcn_raw_buffer_load_b128(rs, (int)(off), 0, aux)
__device__ __forceinline__ u32 lds_ld(const unsigned char* p) { return __hip_atomic_load((const u32*)p, __ATOMIC_RELAXED, __HIP_MEMORY_SCOPE_WORKGROUP); }
__device__ __forceinline__ void lds_spin_ge(const unsigned char* p, u32 want) {
  for (u32 spins = 0; (u32)__builtin_amdgcn_readfirstlane(lds_ld(p)) < want && spins < (1u << 24); ++spins) { }
  asm volatile("" ::: "memory");
}
__device__ __forceinline__ void lds_wait_ge(const unsigned char* p, u32 want) {
  for (u32 spins = 0; (u32)__builtin_amdgcn_readfirstlane(lds_ld(p)) < want && spins < (1u << 22); ++spins) __builtin_amdgcn_s_sleep(1);
  asm volatile("" ::: "memory");
}
__global__ void __launch_bounds__(NT, 2)
fused_decode_kernel(const u16* x_, const u16* kc_, const u16* vc_, const u16* wq_, const u16* wo_,
                    const u16* w1_, const u16* w2_, u16* out_, unsigned char* ws_, int Sk) {
  __shared__ __attribute__((aligned(16))) unsigned char lds[L_TOTAL];
  const GAS u16* x = (const GAS u16*)x_;
  const int tid = threadIdx.x, lane = tid & 63, wave = __builtin_amdgcn_readfirstlane(tid >> 6);
  const int b = blockIdx.x, g = b & 7, j = b >> 3;
  const int r = lane >> 4, c = lane & 15;
  GAS unsigned char* ws = (GAS unsigned char*)ws_;
  const bool has_ffn = j < 28;
  const int jf = has_ffn ? j : 0;
  const int wv = wave & 3, t = tid & 255;
  const u64 dbg_t0 = __builtin_amdgcn_s_memrealtime();
  u64 dbg_t1 = dbg_t0;
  u32 cpf0 = 0;
  if (wave == 0) {
    const GAS u32* cp = (const GAS u32*)(__builtin_amdgcn_s_getpc() & ~127ull);
    cpf0 = cp[lane * 32];
  }
  if (__builtin_expect(wave >= 4, 1)) {
    u32x4 R[6];
    const u32x4 xv = *(const GAS u32x4*)(x + 8 * lane);
    const u64 E = ld_agent((const GAS u64*)ws);
    const __amdgpu_buffer_rsrc_t rq = MKRS(wq_, D * NQ * 2), r1 = MKRS(w1_, D * DFF * 2);
    const int qoff = ((256 * (j & 1) + (t >> 2)) * NQ + g * 512 + (j >> 1) * 32 + (t & 3) * 8) * 2;
#pragma unroll
    for (int i = 0; i < 4; ++i) R[i] = LDB(rq, qoff + 64 * i * NQ * 2, AUX_STREAM);
    const int o1 = ((64 * g + (t >> 3)) * DFF + 64 * jf + 8 * (t & 7)) * 2;
    R[4] = LDB(r1, o1, AUX_STREAM); R[5] = LDB(r1, o1 + 32 * DFF * 2, AUX_STREAM);
    PIN();
    __builtin_amdgcn_s_barrier();
    PIN();
    float xf[8];
#pragma unroll
    for (int k = 0; k < 4; ++k) { xf[2 * k] = lo16(xv[k]); xf[2 * k + 1] = hi16(xv[k]); }
    float s = 0.f;
#pragma unroll
    for (int k = 0; k < 8; ++k) s += xf[k];
    const float mean = wave_sum(s) * (1.f / D);
    float ss = 0.f;
#pragma unroll
    for (int k = 0; k < 8; ++k) { const float d = xf[k] - mean; ss = fmaf(d, d, ss); }
    const float var = wave_sum(ss) * (1.f / D);
    const float mb = bfr(mean);
    const float rden = __builtin_amdgcn_rcpf(bfr(__builtin_amdgcn_sqrtf(bfr(bfr(var) + 1e-5f))));
#pragma unroll
    for (int k = 0; k < 8; ++k) xf[k] = bfr(bfr(xf[k] - mb) * rden);
    *(f32x4*)(lds + L_XN + lane * 32) = f32x4{xf[0], xf[1], xf[2], xf[3]};
    *(f32x4*)(lds + L_XN + lane * 32 + 16) = f32x4{xf[4], xf[5], xf[6], xf[7]};
    const u64 tagE = E + 1;
    const u32 tlo = (u32)tagE, thi8 = (u32)(tagE >> 32) & 0xffu;
    {
      float v[8];
#pragma unroll
      for (int k = 0; k < 8; ++k) v[k] = 0.f;
#pragma unroll
      for (int i = 0; i < 4; ++i) {
        const float xn = *(const float*)(lds + L_XN + (256 * (j & 1) + (t >> 2) + 64 * i) * 4);
#pragma unroll
        for (int k = 0; k < 4; ++k) { v[2 * k] = fmaf(xn, lo16(R[i][k]), v[2 * k]); v[2 * k + 1] = fmaf(xn, hi16(R[i][k]), v[2 * k + 1]); }
      }
      float q = tr8_hi(v, lane);
      q += __builtin_bit_cast(float, __builtin_amdgcn_ds_swizzle(__builtin_bit_cast(int, q), 0x101F));
      if ((lane & 4) == 0) {
        const int col = (lane & 3) * 8 + (lane >> 5) * 4 + ((lane >> 4) & 1) * 2 + ((lane >> 3) & 1);
        *(float*)(lds + L_QRED + (col * 4 + wv) * 4) = q;
      }
    }
    {
      const float xa = *(const float*)(lds + L_XN + (64 * g + (t >> 3)) * 4), xb = *(const float*)(lds + L_XN + (64 * g + 32 + (t >> 3)) * 4);
      float v[8];
#pragma unroll
      for (int k = 0; k < 4; ++k) { v[2 * k] = fmaf(xa, lo16(R[4][k]), xb * lo16(R[5][k])); v[2 * k + 1] = fmaf(xa, hi16(R[4][k]), xb * hi16(R[5][k])); }
      const float h = tr8_hi(v, lane);
      *(float*)(lds + L_HRED + ((8 * (lane & 7) + (lane >> 5) * 4 + ((lane >> 4) & 1) * 2 + ((lane >> 3) & 1)) * 4 + wv) * 4) = h;
    }
    asm volatile("s_waitcnt lgkmcnt(0)" ::: "memory");
    u32 old = 0;
    if (lane == 0) old = __hip_atomic_fetch_add((u32*)(lds + L_CNT), 1u, __ATOMIC_RELAXED, __HIP_MEMORY_SCOPE_WORKGROUP);
    old = __builtin_amdgcn_readfirstlane(old);
    if (old == 3u) {
      asm volatile("" ::: "memory");
      const f32x4 a = *(const f32x4*)(lds + L_QRED + (lane & 31) * 16);
      if (lane < 32) st_agent((GAS u64*)(ws + WS_Q) + (g * 32 + j) * 32 + lane, pack40((a.x + a.y) + (a.z + a.w), tlo, thi8));
      const f32x4 hh = *(const f32x4*)(lds + L_HRED + lane * 16);
      if (has_ffn) st_agent((GAS u64*)(ws + WS_H) + ((j * 8 + (lane >> 3)) * 8 + g) * 16 + (lane & 7), pack40((hh.x + hh.y) + (hh.z + hh.w), tlo, thi8));
      PIN();
      if (lane == 0) __hip_atomic_store((u32*)(lds + L_CNT + 24), 1u, __ATOMIC_RELAXED, __HIP_MEMORY_SCOPE_WORKGROUP);
    }
    PIN();
    lds_wait_ge(lds + L_CNT + 24, 1u);
    {
      const __amdgpu_buffer_rsrc_t rs = __builtin_amdgcn_make_buffer_rsrc((void*)(ws_ + WS_Q), 0, 8 * 32 * 256, 0x00020000);
      const int off = (g * 32 + (t >> 4) * 2) * 256 + (t & 15) * 16;
      u32x4 a[1], bq[1];
#define QPOLL(k) do { asm volatile("" ::: "memory"); a[k] = __builtin_amdgcn_raw_buffer_load_b128(rs, off, 0, 16); bq[k] = __builtin_amdgcn_raw_buffer_load_b128(rs, off + 256, 0, 16); PIN(); } while (0)
#define QBAD(v) ((v.x ^ tlo) | ((v.y ^ thi8) & 0xffu) | (v.z ^ tlo) | ((v.w ^ thi8) & 0xffu))
#define QOK(k) __all((QBAD(a[k]) | QBAD(bq[k])) == 0)
#define QDONE(k) do { qpk = pk2(val40(a[k].y) + val40(bq[k].y), val40(a[k].w) + val40(bq[k].w)); } while (0)
      u32 qpk = 0;
      lds_wait_ge(lds + L_CNT + 16, 4u);
      QPOLL(0);
      for (u32 spins = 0; !QOK(0) && spins < SPIN_CAP; ++spins) QPOLL(0);
      QDONE(0);
      *(u32*)(lds + L_QS + t * 4) = qpk;
      asm volatile("s_waitcnt lgkmcnt(0)" ::: "memory");
      if (lane == 0) __hip_atomic_fetch_add((u32*)(lds + L_CNT + 4), 1u, __ATOMIC_RELAXED, __HIP_MEMORY_SCOPE_WORKGROUP);
    }
    PIN();
    return;
  }
  if (tid < 8) *(u32*)(lds + L_CNT + tid * 4) = 0u;
  const u64 E = ld_agent((const GAS u64*)ws);
  asm volatile("s_waitcnt lgkmcnt(0)" ::: "memory");
  PIN();
  __builtin_amdgcn_s_barrier();
  PIN();
  u32x4 Kf[8], Vc[8];
  const size_t kvbase = ((size_t)g * Sk + (Sk - WIN) + j * 128 + wv * 32) * DH;
  u32x4 Wo[4], W2[2];
  const __amdgpu_buffer_rsrc_t r2 = MKRS(w2_, DFF * D * 2), rwo = MKRS(wo_, NQ * D * 2);
  const int o2 = ((64 * jf + 8 * g + r) * D + 128 * wv + 8 * c) * 2, oo = (((g * 32 + j) * 16 + r) * D + 128 * wv + 8 * c) * 2;
  const __amdgpu_buffer_rsrc_t rk = MKRS(kc_ + kvbase, 32 * DH * 2), rv = MKRS(vc_ + kvbase, 32 * DH * 2);
#pragma unroll
  for (int i = 0; i < 4; ++i) Wo[i] = LDB(rwo, oo + 4 * i * D * 2, AUX_STREAM);
  W2[0] = LDB(r2, o2, AUX_STREAM); W2[1] = LDB(r2, o2 + 4 * D * 2, AUX_STREAM);
  u32 tch = 0, tch2 = 0, tch3 = 0;
  if (lane == 0) {
    tch = *(const GAS u32*)((const GAS u16*)kc_ + kvbase); tch2 = *(const GAS u32*)((const GAS u16*)vc_ + kvbase);
    tch3 = *(const GAS u32*)(wv == 0 ? (const GAS unsigned char*)out_ : ws + WS_END + 64);
  }
  {
    const u64 t0 = __builtin_amdgcn_s_memrealtime();
    while ((u32)__builtin_amdgcn_readfirstlane(lds_ld(lds + L_CNT + 24)) < 1u && __builtin_amdgcn_s_memrealtime() - t0 < KV_DEADLINE) __builtin_amdgcn_s_sleep(1);
    asm volatile("" ::: "memory");
  }
  PIN(); dbg_t1 = __builtin_amdgcn_s_memrealtime(); PIN();
#pragma unroll
  for (int i = 0; i < 4; ++i) Kf[i] = LDB(rk, ((4 * i + r) * DH + 8 * c) * 2, AUX_STREAM);
  PIN();
#pragma unroll
  for (int i = 4; i < 8; ++i) Kf[i] = LDB(rk, ((4 * i + r) * DH + 8 * c) * 2, AUX_STREAM);
  PIN();
#pragma unroll
  for (int i = 0; i < 8; ++i) Vc[i] = LDB(rv, ((4 * i + r) * DH + 8 * c) * 2, AUX_STREAM);
  PIN();
  const GAS u64* hp = (const GAS u64*)(ws + WS_H) + ((jf * 8 + g) * 8 + (lane >> 3)) * 16 + (lane & 7);
  PIN();
  const u64 tagE = E + 1;
  const u32 tlo = (u32)tagE, thi8 = (u32)(tagE >> 32) & 0xffu;
  asm volatile("" :: "v"(tch), "v"(tch2), "v"(tch3));
  asm volatile("" :: "v"(cpf0));
#pragma unroll
  for (int i = 0; i < 8; ++i) *(u32x4*)(lds + L_KT + voff(32 * wv + 4 * i + r, c)) = Kf[i];
  PIN();
  if (lane == 0) __hip_atomic_fetch_add((u32*)(lds + L_CNT + 16), 1u, __ATOMIC_RELAXED, __HIP_MEMORY_SCOPE_WORKGROUP);
  u64 hg = ld_agent(hp);
  PIN();
#pragma unroll
  for (int i = 0; i < 8; ++i) Kf[i] = *(const u32x4*)(lds + L_KT + voff(32 * wv + (i >> 2) * 16 + c, 4 * (i & 3) + r));
  PIN();
#pragma unroll
  for (int i = 0; i < 8; ++i) *(u32x4*)(lds + L_VT + voff(32 * wv + 4 * i + r, c)) = Vc[i];
  u32x4 Vf[8];
  {
    const int q4 = c >> 2, p4 = c & 3;
#pragma unroll
    for (int n = 0; n < 8; ++n) {
      const u32x2 w0 = __builtin_bit_cast(u32x2, __builtin_amdgcn_ds_read_tr16_b64_v4i16((LAS s16x4*)(lds + L_VT + voff(32 * wv + 4 * r + q4, 2 * n + (p4 >> 1)) + 8 * (p4 & 1))));
      const u32x2 w1 = __builtin_bit_cast(u32x2, __builtin_amdgcn_ds_read_tr16_b64_v4i16((LAS s16x4*)(lds + L_VT + voff(32 * wv + 16 + 4 * r + q4, 2 * n + (p4 >> 1)) + 8 * (p4 & 1))));
      Vf[n] = u32x4{w0.x, w0.y, w1.x, w1.y};
    }
  }
  float y[8];
  {
    if (has_ffn) {
      for (u32 spins = 0; !__all(bad40(hg, tlo, thi8) == 0) && spins < SPIN_CAP; ++spins) hg = ld_agent(hp);
    }
    float hv = has_ffn ? val40((u32)(hg >> 32)) : 0.f;
    hv += dpp<XOR8>(hv); hv = xrow_sum(hv);
    hv = fmaxf(bfr(hv), 0.f);
    const float h0 = __shfl(hv, r), h1 = __shfl(hv, r + 4);
#pragma unroll
    for (int k = 0; k < 4; ++k) {
      y[2 * k] = fmaf(h0, lo16(W2[0][k]), h1 * lo16(W2[1][k]));
      y[2 * k + 1] = fmaf(h0, hi16(W2[0][k]), h1 * hi16(W2[1][k]));
    }
  }
  PIN();
  lds_spin_ge(lds + L_CNT + 4, 4u);
  {
    f32x4 a0 = {0.f, 0.f, 0.f, 0.f}, a1 = {0.f, 0.f, 0.f, 0.f};
#pragma unroll
    for (int i = 0; i < 4; ++i) {
      const u32x4 qf = *(const u32x4*)(lds + L_QS + (c & 3) * 256 + 64 * i + 16 * r);
      a0 = mfma16(Kf[i], qf, a0);
      a1 = mfma16(Kf[4 + i], qf, a1);
    }
    float sc[8];
#pragma unroll
    for (int i = 0; i < 4; ++i) { sc[i] = bfr(bfr(a0[i]) * 0.08838834764831845f); sc[4 + i] = bfr(bfr(a1[i]) * 0.08838834764831845f); }
    const float mw = xrow_max(fmaxf(fmaxf(fmaxf(sc[0], sc[1]), fmaxf(sc[2], sc[3])), fmaxf(fmaxf(sc[4], sc[5]), fmaxf(sc[6], sc[7]))));
#pragma unroll
    for (int i = 0; i < 8; ++i) sc[i] = __builtin_amdgcn_exp2f((sc[i] - mw) * LOG2E);
    const u32x4 pf = {pk2(sc[0], sc[1]), pk2(sc[2], sc[3]), pk2(sc[4], sc[5]), pk2(sc[6], sc[7])};
    const u32x4 ones = {0x3f803f80u, 0x3f803f80u, 0x3f803f80u, 0x3f803f80u};
    const f32x4 z = {0.f, 0.f, 0.f, 0.f};
    float ow[8];
#pragma unroll
    for (int n = 0; n < 8; ++n) ow[n] = sel4(mfma16(pf, Vf[n], z), r);
    const f32x4 lac = mfma16(pf, ones, z);
    *(f32x4*)(lds + L_OW + ((wv * 4 + r) * 16 + c) * 32) = f32x4{ow[0], ow[1], ow[2], ow[3]};
    *(f32x4*)(lds + L_OW + ((wv * 4 + r) * 16 + c) * 32 + 16) = f32x4{ow[4], ow[5], ow[6], ow[7]};
    if (lane < 4) { *(float*)(lds + L_WML + (lane * 4 + wv) * 4) = mw; *(float*)(lds + L_WML + 64 + (lane * 4 + wv) * 4) = sel4(lac, lane); }
  }
  __syncthreads();
  {
    const f32x4 m4 = *(const f32x4*)(lds + L_WML + r * 16), l4 = *(const f32x4*)(lds + L_WML + 64 + r * 16);
    f32x2 pv[4];
#pragma unroll
    for (int w = 0; w < 4; ++w) pv[w] = *(const f32x2*)(lds + L_OW + ((w * 4 + r) * 16 + c) * 32 + wv * 8);
    const float M = fmaxf(fmaxf(m4.x, m4.y), fmaxf(m4.z, m4.w));
    float e[4];
#pragma unroll
    for (int w = 0; w < 4; ++w) e[w] = __builtin_amdgcn_exp2f((m4[w] - M) * LOG2E);
    const float L = fmaf(e[0], l4.x, fmaf(e[1], l4.y, fmaf(e[2], l4.z, e[3] * l4.w)));
    const float rl = __builtin_amdgcn_rcpf(L);
    const float x0 = fmaf(e[0], pv[0].x, fmaf(e[1], pv[1].x, fmaf(e[2], pv[2].x, e[3] * pv[3].x))) * rl;
    const float x1 = fmaf(e[0], pv[0].y, fmaf(e[1], pv[1].y, fmaf(e[2], pv[2].y, e[3] * pv[3].y))) * rl;
    const float y0 = dpp<XOR1>(x0), y1 = dpp<XOR1>(x1);
    const bool odd = c & 1;
    const u32 pr = odd ? pk2(y1, x1) : pk2(x0, y0);
    const u64 lg = pack40(fmaf(M, LOG2E, __builtin_amdgcn_logf(L)), tlo, thi8);
    const __amdgpu_buffer_rsrc_t ro = __builtin_amdgcn_make_buffer_rsrc((void*)(ws_ + WS_O), 0, (int)(256 * 512 * 8), 0x00020000);
    __builtin_amdgcn_raw_buffer_store_b128(u32x4{pr, tlo, (u32)lg, (u32)(lg >> 32)}, ro, ((g * 32 + r * 8 + 2 * wv + (c & 1)) * 32 + j) * 128 + (c >> 1) * 16, 0, 16);
  }
  PIN();
  {
    const __amdgpu_buffer_rsrc_t ro = __builtin_amdgcn_make_buffer_rsrc((void*)(ws_ + WS_O), 0, (int)(256 * 512 * 8), 0x00020000);
    const int off = (g * 32 + j) * 4096 + t * 16;
    u32x4 og;
    f32x2 wf[16];
#pragma unroll
    for (int i = 0; i < 4; ++i)
#pragma unroll
      for (int k = 0; k < 4; ++k) { wf[4 * i + k] = f32x2{lo16(Wo[i][k]), hi16(Wo[i][k])}; asm volatile("" : "+v"(wf[4 * i + k])); }
    __builtin_amdgcn_s_sleep(POLL_DELAY);
    for (u32 spins = 0;; ++spins) {
      asm volatile("" ::: "memory");
      og = __builtin_amdgcn_raw_buffer_load_b128(ro, off, 0, 16);
      const u32 bad = (og.y ^ tlo) | (og.z ^ tlo) | ((og.w ^ thi8) & 0xffu);
      if (__all(bad == 0) || spins >= SPIN_CAP) break;
    }
    const float lv = val40(og.w);
    float Mw = lv;
    Mw = fmaxf(Mw, dpp<XOR8>(Mw)); Mw = xrow_max(Mw);
    const float mine = __builtin_amdgcn_exp2f(lv - Mw);
    float Ww = mine;
    Ww += dpp<XOR8>(Ww); Ww = xrow_sum(Ww);
    float v = tr32(mine * lo16(og.x), mine * hi16(og.x));
    { auto tt = __builtin_amdgcn_permlane16_swap(__float_as_uint(v), __float_as_uint(v), false, false); v = __uint_as_float(tt[0]) + __uint_as_float(tt[1]); }
    v += dpp<XOR8>(v);
    if ((lane & 24) == 0) *(float*)(lds + L_ORED + ((2 * (lane & 7) + (lane >> 5)) * 4 + wv) * 4) = v;
    if (lane == 8) { *(float*)(lds + L_HV + wv * 4) = Mw; *(float*)(lds + L_HV + 16 + wv * 4) = Ww; }
    __syncthreads();
    const f32x4 m4 = *(const f32x4*)(lds + L_HV), w4 = *(const f32x4*)(lds + L_HV + 16);
    const float Mx = fmaxf(fmaxf(m4.x, m4.y), fmaxf(m4.z, m4.w));
    f32x4 e4;
#pragma unroll
    for (int w = 0; w < 4; ++w) e4[w] = __builtin_amdgcn_exp2f(m4[w] - Mx);
    const float rL = __builtin_amdgcn_rcpf(fmaf(e4.x, w4.x, fmaf(e4.y, w4.y, fmaf(e4.z, w4.z, e4.w * w4.w))));
#pragma unroll
    for (int i = 0; i < 4; ++i) {
      const f32x4 a0 = *(const f32x4*)(lds + L_ORED + (r + 4 * i) * 16);
      const float o = bfr(fmaf(e4.x, a0.x, fmaf(e4.y, a0.y, fmaf(e4.z, a0.z, e4.w * a0.w))) * rL);
#pragma unroll
      for (int k = 0; k < 4; ++k) { y[2 * k] = fmaf(o, wf[4 * i + k].x, y[2 * k]); y[2 * k + 1] = fmaf(o, wf[4 * i + k].y, y[2 * k + 1]); }
    }
    const float a0 = tr32(y[0], y[4]), a1 = tr32(y[1], y[5]), a2 = tr32(y[2], y[6]), a3 = tr32(y[3], y[7]);
    const float b0 = tr16(a0, a2), b1 = tr16(a1, a3);
    const u64 g0 = pack40(b0, tlo, thi8), g1 = pack40(b1, tlo, thi8);
    const __amdgpu_buffer_rsrc_t rf = __builtin_amdgcn_make_buffer_rsrc((void*)(ws_ + WS_F), 0, (int)(256 * 512 * 8), 0x00020000);
    const int offf = (((16 * wv + c) * 256 + b) * CPR + (lane >> 5) * 4 + ((lane >> 4) & 1) * 2) * 8;
    __builtin_amdgcn_raw_buffer_store_b128(u32x4{(u32)g0, (u32)(g0 >> 32), (u32)g1, (u32)(g1 >> 32)}, rf, offf, 0, 16);
  }
  PIN();
  if (b >= NRED) return;
  {
    const __amdgpu_buffer_rsrc_t rs = __builtin_amdgcn_make_buffer_rsrc((void*)(ws_ + WS_F), 0, (int)(256 * 512 * 8), 0x00020000);
    const int off = b * 16384 + tid * 16;
    const u32 xo = x[b * CPR + (tid & 7)];
    __builtin_amdgcn_s_sleep(POLL_DELAY);
    u32x4 fg[4];
    for (u32 spins = 0;; ++spins) {
      asm volatile("" ::: "memory");
#pragma unroll
      for (int k = 0; k < 4; ++k) fg[k] = __builtin_amdgcn_raw_buffer_load_b128(rs, off + 4096 * k, 0, 16);
      u32 bad = 0;
#pragma unroll
      for (int k = 0; k < 4; ++k) bad |= (fg[k].x ^ tlo) | ((fg[k].y ^ thi8) & 0xffu) | (fg[k].z ^ tlo) | ((fg[k].w ^ thi8) & 0xffu);
      if (__all(bad == 0) || spins >= SPIN_CAP) break;
    }
    const float s0 = (val40(fg[0].y) + val40(fg[1].y)) + (val40(fg[2].y) + val40(fg[3].y));
    const float s1 = (val40(fg[0].w) + val40(fg[1].w)) + (val40(fg[2].w) + val40(fg[3].w));
    float s = tr32(s0, s1);
    { auto tt = __builtin_amdgcn_permlane16_swap(__float_as_uint(s), __float_as_uint(s), false, false); s = __uint_as_float(tt[0]) + __uint_as_float(tt[1]); }
    s += dpp<XOR8>(s);
    s += __builtin_bit_cast(float, __builtin_amdgcn_ds_swizzle(__builtin_bit_cast(int, s), 0x101F));
    if ((lane & 28) == 0) *(float*)(lds + L_FRED + ((2 * (lane & 3) + (lane >> 5)) * 4 + wave) * 4) = s;
    __syncthreads();
    if (tid < CPR) {
      const f32x4 a = *(const f32x4*)(lds + L_FRED + tid * 16);
      ((GAS u16*)out_)[b * CPR + tid] = (u16)bfbits(lo16(xo) + ((a.x + a.y) + (a.z + a.w)));
    }
    if (b == 0 && tid == 0) st_agent((GAS u64*)ws, tagE);
    if (b == 0) { const u64 te = __builtin_amdgcn_s_memrealtime(); const u64 iv = (dbg_t1) - dbg_t0; while (__builtin_amdgcn_s_memrealtime() - te < 4 * iv) __builtin_amdgcn_s_sleep(1); }
  }
}

}

extern "C" void kernel_launch(void* const* d_in, const int* in_sizes, int n_in,
                              void* d_out, int out_size, void* d_ws, size_t ws_size,
                              hipStream_t stream) {
  const u16* x = (const u16*)d_in[0];
  const u16* kc = (const u16*)d_in[1];
  const u16* vc = (const u16*)d_in[2];
  const u16* wq = (const u16*)d_in[3];
  const u16* wo = (const u16*)d_in[4];
  const u16* w1 = (const u16*)d_in[6];
  const u16* w2 = (const u16*)d_in[7];
  const int Sk = in_sizes[1] / (HKV * DH);
  fused_decode_kernel<<<NWG, NT, 0, stream>>>(x, kc, vc, wq, wo, w1, w2, (u16*)d_out, (unsigned char*)d_ws, Sk);
}
```

```cpp
#include <hip/hip_runtime.h>
#include <hip/hip_bf16.h>
#include <stdint.h>

typedef unsigned u32;
typedef unsigned long long u64;
typedef unsigned short u16;
typedef __attribute__((ext_vector_type(4))) unsigned u32x4;
typedef __attribute__((ext_vector_type(2))) unsigned u32x2;
typedef __attribute__((ext_vector_type(4))) float f32x4;
typedef __attribute__((ext_vector_type(2))) float f32x2;
typedef __attribute__((ext_vector_type(8))) short s16x8;
typedef __attribute__((ext_vector_type(4))) short s16x4;
typedef __attribute__((ext_vector_type(2))) __bf16 bf16x2;
typedef __attribute__((ext_vector_type(8))) __bf16 bf16x8;
#define GAS __attribute__((address_space(1)))
#define LAS __attribute__((address_space(3)))

namespace {

constexpr int D = 512, HKV = 8, DH = 128, NQ = 4096, DFF = 1792, WIN = 4096;
constexpr int NWG = 256, NT = 512;
constexpr int NRED = 64, CPR = D / NRED;
constexpr size_t WS_Q = 4096;
constexpr size_t WS_H = WS_Q + 8 * 32 * 256;
constexpr size_t WS_O = WS_H + 32 * 8 * 8 * 128;
constexpr size_t WS_F = WS_O + (size_t)8 * 32 * 32 * 16 * 8;
constexpr size_t WS_END = WS_F + (size_t)256 * 512 * 8;
constexpr u32 SPIN_CAP = 1u << 16;
constexpr float LOG2E = 1.4426950408889634f;

constexpr int L_VT = 0;
constexpr int L_OW = 32768;
constexpr int L_QS = 40960;
constexpr int L_QRED = 46080;
constexpr int L_WML = 42240;
constexpr int L_ORED = 42496;
constexpr int L_FRED = 42752;
constexpr int L_HV = 42400;
constexpr int L_CNT = 42880;
constexpr int L_XN = 42912;
constexpr int L_HRED = 44960;
constexpr int L_KT = 46592;
constexpr int L_TOTAL = L_KT + 32768;

__device__ __forceinline__ float bfr(float f) { return (float)(__bf16)f; }
__device__ __forceinline__ float lo16(u32 u) { return __uint_as_float(u << 16); }
__device__ __forceinline__ float hi16(u32 u) { return __uint_as_float(u & 0xffff0000u); }
__device__ __forceinline__ u32 bfbits(float f) { return (u32)__builtin_bit_cast(u16, (__bf16)f); }
__device__ __forceinline__ u32 pk2(float a, float b) { bf16x2 v = {(__bf16)a, (__bf16)b}; return __builtin_bit_cast(u32, v); }
template <int CTRL> __device__ __forceinline__ float dpp(float x) {
  return __builtin_bit_cast(float, __builtin_amdgcn_mov_dpp(__builtin_bit_cast(int, x), CTRL, 0xf, 0xf, true));
}
constexpr int XOR1 = 0xB1, XOR2 = 0x4E, XOR7 = 0x141, XOR8 = 0x128;
__device__ __forceinline__ float xrow_max(float x) {
  auto s = __builtin_amdgcn_permlane16_swap(__float_as_uint(x), __float_as_uint(x), false, false);
  x = fmaxf(__uint_as_float(s[0]), __uint_as_float(s[1]));
  auto t = __builtin_amdgcn_permlane32_swap(__float_as_uint(x), __float_as_uint(x), false, false);
  return fmaxf(__uint_as_float(t[0]), __uint_as_float(t[1]));
}
__device__ __forceinline__ float xrow_sum(float x) {
  auto s = __builtin_amdgcn_permlane16_swap(__float_as_uint(x), __float_as_uint(x), false, false);
  x = __uint_as_float(s[0]) + __uint_as_float(s[1]);
  auto t = __builtin_amdgcn_permlane32_swap(__float_as_uint(x), __float_as_uint(x), false, false);
  return __uint_as_float(t[0]) + __uint_as_float(t[1]);
}
__device__ __forceinline__ float wave_sum(float v) {
  v += dpp<XOR1>(v); v += dpp<XOR2>(v); v += dpp<XOR7>(v); v += dpp<XOR8>(v);
  return xrow_sum(v);
}
__device__ __forceinline__ float tr32(float a, float b) {
  auto t = __builtin_amdgcn_permlane32_swap(__float_as_uint(a), __float_as_uint(b), false, false);
  return __uint_as_float(t[0]) + __uint_as_float(t[1]);
}
__device__ __forceinline__ float tr16(float a, float b) {
  auto t = __builtin_amdgcn_permlane16_swap(__float_as_uint(a), __float_as_uint(b), false, false);
  return __uint_as_float(t[0]) + __uint_as_float(t[1]);
}
template <int CTRL> __device__ __forceinline__ float trd(float a, float b, bool up) {
  const float send = up ? a : b, keep = up ? b : a;
  return keep + dpp<CTRL>(send);
}
__device__ __forceinline__ float tr8_hi(const float* v, int lane) {
  const float a0 = tr32(v[0], v[4]), a1 = tr32(v[1], v[5]), a2 = tr32(v[2], v[6]), a3 = tr32(v[3], v[7]);
  const float b0 = tr16(a0, a2), b1 = tr16(a1, a3);
  return trd<XOR8>(b0, b1, lane & 8);
}
__device__ __forceinline__ float sel4(f32x4 a, int i) {
  const float lo = (i & 1) ? a[1] : a[0], hi = (i & 1) ? a[3] : a[2];
  return (i & 2) ? hi : lo;
}
__device__ __forceinline__ f32x4 mfma16(u32x4 a, u32x4 b, f32x4 c) {
  return __builtin_amdgcn_mfma_f32_16x16x32_bf16(__builtin_bit_cast(bf16x8, a), __builtin_bit_cast(bf16x8, b), c, 0, 0, 0);
}
__device__ __forceinline__ u64 ld_agent(const GAS u64* p) { return __hip_atomic_load(p, __ATOMIC_RELAXED, __HIP_MEMORY_SCOPE_AGENT); }
__device__ __forceinline__ void st_agent(GAS u64* p, u64 v) { __hip_atomic_store(p, v, __ATOMIC_RELAXED, __HIP_MEMORY_SCOPE_AGENT); }
__device__ __forceinline__ u64 pack40(float v, u32 tlo, u32 thi8) {
  const u32 b = __float_as_uint(v);
  const u32 rr = b + 0x7fu + ((b >> 8) & 1u), rn = b | 0x00400000u;
  const u32 m = (v != v) ? 0xffffffffu : 0u;
  u32 r = (rn & m) | (rr & ~m);
  r = (r & 0xffffff00u) | thi8;
  return ((u64)r << 32) | tlo;
}
__device__ __forceinline__ u32 bad40(u64 g, u32 tlo, u32 thi8) { return ((u32)g ^ tlo) | (((u32)(g >> 32) ^ thi8) & 0xffu); }
__device__ __forceinline__ float val40(u32 hi) { return __uint_as_float(hi & 0xffffff00u); }
__device__ __forceinline__ u32 voff(u32 row, u32 ch) { return 256u * row + 16u * (ch ^ (((row & 3u) << 2) | ((row >> 2) & 3u))); }
#define PIN() do { asm volatile("" ::: "memory"); __builtin_amdgcn_sched_barrier(0); } while (0)

constexpr int AUX_STREAM = 18;
constexpr int POLL_DELAY = 30;
#define MKRS(p, n) __builtin_amdgcn_make_buffer_rsrc((void*)(p), 0, (int)(n), 0x00020000)
#define LDB(rs, off, aux) __builtin_amdgcn_raw_buffer_load_b128(rs, (int)(off), 0, aux)
__device__ __forceinline__ u32 lds_ld(const unsigned char* p) { return __hip_atomic_load((const u32*)p, __ATOMIC_RELAXED, __HIP_MEMORY_SCOPE_WORKGROUP); }
__device__ __forceinline__ void lds_spin_ge(const unsigned char* p, u32 want) {
  for (u32 spins = 0; (u32)__builtin_amdgcn_readfirstlane(lds_ld(p)) < want && spins < (1u << 24); ++spins) { }
  asm volatile("" ::: "memory");
}
__device__ __forceinline__ void lds_wait_ge(const unsigned char* p, u32 want) {
  for (u32 spins = 0; (u32)__builtin_amdgcn_readfirstlane(lds_ld(p)) < want && spins < (1u << 22); ++spins) __builtin_amdgcn_s_sleep(1);
  asm volatile("" ::: "memory");
}
__global__ void __launch_bounds__(NT, 2)
fused_decode_kernel(const u16* x_, const u16* kc_, const u16* vc_, const u16* wq_, const u16* wo_,
                    const u16* w1_, const u16* w2_, u16* out_, unsigned char* ws_, int Sk) {
  __shared__ __attribute__((aligned(16))) unsigned char lds[L_TOTAL];
  const GAS u16* x = (const GAS u16*)x_;
  const int tid = threadIdx.x, lane = tid & 63, wave = __builtin_amdgcn_readfirstlane(tid >> 6);
  const int b = blockIdx.x, g = b & 7, j = b >> 3;
  const int r = lane >> 4, c = lane & 15;
  GAS unsigned char* ws = (GAS unsigned char*)ws_;
  const bool has_ffn = j < 28;
  const int jf = has_ffn ? j : 0;
  const int wv = wave & 3, t = tid & 255;
  u32 cpf0 = 0;
  if (wave == 0) {
    const GAS u32* cp = (const GAS u32*)(__builtin_amdgcn_s_getpc() & ~127ull);
    cpf0 = cp[lane * 32];
  }
  if (__builtin_expect(wave >= 4, 1)) {
    u32x4 R[6];
    const u32x4 xv = *(const GAS u32x4*)(x + 8 * lane);
    const u64 E = ld_agent((const GAS u64*)ws);
    const __amdgpu_buffer_rsrc_t rq = MKRS(wq_, D * NQ * 2), r1 = MKRS(w1_, D * DFF * 2);
    const int qoff = ((256 * (j & 1) + (t >> 2)) * NQ + g * 512 + (j >> 1) * 32 + (t & 3) * 8) * 2;
#pragma unroll
    for (int i = 0; i < 4; ++i) R[i] = LDB(rq, qoff + 64 * i * NQ * 2, AUX_STREAM);
    const int o1 = ((64 * g + (t >> 3)) * DFF + 64 * jf + 8 * (t & 7)) * 2;
    R[4] = LDB(r1, o1, AUX_STREAM); R[5] = LDB(r1, o1 + 32 * DFF * 2, AUX_STREAM);
    PIN();
    __builtin_amdgcn_s_barrier();
    PIN();
    float xf[8];
#pragma unroll
    for (int k = 0; k < 4; ++k) { xf[2 * k] = lo16(xv[k]); xf[2 * k + 1] = hi16(xv[k]); }
    float s = 0.f;
#pragma unroll
    for (int k = 0; k < 8; ++k) s += xf[k];
    const float mean = wave_sum(s) * (1.f / D);
    float ss = 0.f;
#pragma unroll
    for (int k = 0; k < 8; ++k) { const float d = xf[k] - mean; ss = fmaf(d, d, ss); }
    const float var = wave_sum(ss) * (1.f / D);
    const float mb = bfr(mean);
    const float rden = __builtin_amdgcn_rcpf(bfr(__builtin_amdgcn_sqrtf(bfr(bfr(var) + 1e-5f))));
#pragma unroll
    for (int k = 0; k < 8; ++k) xf[k] = bfr(bfr(xf[k] - mb) * rden);
    *(f32x4*)(lds + L_XN + lane * 32) = f32x4{xf[0], xf[1], xf[2], xf[3]};
    *(f32x4*)(lds + L_XN + lane * 32 + 16) = f32x4{xf[4], xf[5], xf[6], xf[7]};
    const u64 tagE = E + 1;
    const u32 tlo = (u32)tagE, thi8 = (u32)(tagE >> 32) & 0xffu;
    {
      float v[8];
#pragma unroll
      for (int k = 0; k < 8; ++k) v[k] = 0.f;
#pragma unroll
      for (int i = 0; i < 4; ++i) {
        const float xn = *(const float*)(lds + L_XN + (256 * (j & 1) + (t >> 2) + 64 * i) * 4);
#pragma unroll
        for (int k = 0; k < 4; ++k) { v[2 * k] = fmaf(xn, lo16(R[i][k]), v[2 * k]); v[2 * k + 1] = fmaf(xn, hi16(R[i][k]), v[2 * k + 1]); }
      }
      float q = tr8_hi(v, lane);
      q += __builtin_bit_cast(float, __builtin_amdgcn_ds_swizzle(__builtin_bit_cast(int, q), 0x101F));
      if ((lane & 4) == 0) {
        const int col = (lane & 3) * 8 + (lane >> 5) * 4 + ((lane >> 4) & 1) * 2 + ((lane >> 3) & 1);
        *(float*)(lds + L_QRED + (col * 4 + wv) * 4) = q;
      }
    }
    {
      const float xa = *(const float*)(lds + L_XN + (64 * g + (t >> 3)) * 4), xb = *(const float*)(lds + L_XN + (64 * g + 32 + (t >> 3)) * 4);
      float v[8];
#pragma unroll
      for (int k = 0; k < 4; ++k) { v[2 * k] = fmaf(xa, lo16(R[4][k]), xb * lo16(R[5][k])); v[2 * k + 1] = fmaf(xa, hi16(R[4][k]), xb * hi16(R[5][k])); }
      const float h = tr8_hi(v, lane);
      *(float*)(lds + L_HRED + ((8 * (lane & 7) + (lane >> 5) * 4 + ((lane >> 4) & 1) * 2 + ((lane >> 3) & 1)) * 4 + wv) * 4) = h;
    }
    asm volatile("s_waitcnt lgkmcnt(0)" ::: "memory");
    u32 old = 0;
    if (lane == 0) old = __hip_atomic_fetch_add((u32*)(lds + L_CNT), 1u, __ATOMIC_RELAXED, __HIP_MEMORY_SCOPE_WORKGROUP);
    old = __builtin_amdgcn_readfirstlane(old);
    if (old == 3u) {
      asm volatile("" ::: "memory");
      const f32x4 a = *(const f32x4*)(lds + L_QRED + (lane & 31) * 16);
      if (lane < 32) st_agent((GAS u64*)(ws + WS_Q) + (g * 32 + j) * 32 + lane, pack40((a.x + a.y) + (a.z + a.w), tlo, thi8));
      const f32x4 hh = *(const f32x4*)(lds + L_HRED + lane * 16);
      if (has_ffn) st_agent((GAS u64*)(ws + WS_H) + ((j * 8 + (lane >> 3)) * 8 + g) * 16 + (lane & 7), pack40((hh.x + hh.y) + (hh.z + hh.w), tlo, thi8));
      PIN();
      if (lane == 0) __hip_atomic_store((u32*)(lds + L_CNT + 24), 1u, __ATOMIC_RELAXED, __HIP_MEMORY_SCOPE_WORKGROUP);
    }
    PIN();
    lds_wait_ge(lds + L_CNT + 24, 1u);
    {
      const __amdgpu_buffer_rsrc_t rs = __builtin_amdgcn_make_buffer_rsrc((void*)(ws_ + WS_Q), 0, 8 * 32 * 256, 0x00020000);
      const int off = (g * 32 + (t >> 4) * 2) * 256 + (t & 15) * 16;
      u32x4 a[1], bq[1];
#define QPOLL(k) do { asm volatile("" ::: "memory"); a[k] = __builtin_amdgcn_raw_buffer_load_b128(rs, off, 0, 16); bq[k] = __builtin_amdgcn_raw_buffer_load_b128(rs, off + 256, 0, 16); PIN(); } while (0)
#define QBAD(v) ((v.x ^ tlo) | ((v.y ^ thi8) & 0xffu) | (v.z ^ tlo) | ((v.w ^ thi8) & 0xffu))
#define QOK(k) __all((QBAD(a[k]) | QBAD(bq[k])) == 0)
#define QDONE(k) do { qpk = pk2(val40(a[k].y) + val40(bq[k].y), val40(a[k].w) + val40(bq[k].w)); } while (0)
      u32 qpk = 0;
      lds_wait_ge(lds + L_CNT + 16, 4u);
      QPOLL(0);
      for (u32 spins = 0; !QOK(0) && spins < SPIN_CAP; ++spins) QPOLL(0);
      QDONE(0);
      *(u32*)(lds + L_QS + t * 4) = qpk;
      asm volatile("s_waitcnt lgkmcnt(0)" ::: "memory");
      if (lane == 0) __hip_atomic_fetch_add((u32*)(lds + L_CNT + 4), 1u, __ATOMIC_RELAXED, __HIP_MEMORY_SCOPE_WORKGROUP);
    }
    PIN();
    return;
  }
  if (tid < 8) *(u32*)(lds + L_CNT + tid * 4) = 0u;
  const u64 E = ld_agent((const GAS u64*)ws);
  asm volatile("s_waitcnt lgkmcnt(0)" ::: "memory");
  PIN();
  __builtin_amdgcn_s_barrier();
  PIN();
  u32x4 Kf[8], Vc[8];
  const size_t kvbase = ((size_t)g * Sk + (Sk - WIN) + j * 128 + wv * 32) * DH;
  u32x4 Wo[4], W2[2];
  const __amdgpu_buffer_rsrc_t r2 = MKRS(w2_, DFF * D * 2), rwo = MKRS(wo_, NQ * D * 2);
  const int o2 = ((64 * jf + 8 * g + r) * D + 128 * wv + 8 * c) * 2, oo = (((g * 32 + j) * 16 + r) * D + 128 * wv + 8 * c) * 2;
  const __amdgpu_buffer_rsrc_t rk = MKRS(kc_ + kvbase, 32 * DH * 2), rv = MKRS(vc_ + kvbase, 32 * DH * 2);
#pragma unroll
  for (int i = 0; i < 4; ++i) Wo[i] = LDB(rwo, oo + 4 * i * D * 2, AUX_STREAM);
  W2[0] = LDB(r2, o2, AUX_STREAM); W2[1] = LDB(r2, o2 + 4 * D * 2, AUX_STREAM);
  u32 tch = 0, tch2 = 0, tch3 = 0;
  if (lane == 0) {
    tch = *(const GAS u32*)((const GAS u16*)kc_ + kvbase); tch2 = *(const GAS u32*)((const GAS u16*)vc_ + kvbase);
    tch3 = *(const GAS u32*)(wv == 0 ? (const GAS unsigned char*)out_ : ws + WS_END + 64);
  }
  lds_wait_ge(lds + L_CNT + 24, 1u);
  PIN();
#pragma unroll
  for (int i = 0; i < 4; ++i) Kf[i] = LDB(rk, ((4 * i + r) * DH + 8 * c) * 2, AUX_STREAM);
  PIN();
#pragma unroll
  for (int i = 4; i < 8; ++i) Kf[i] = LDB(rk, ((4 * i + r) * DH + 8 * c) * 2, AUX_STREAM);
  PIN();
#pragma unroll
  for (int i = 0; i < 8; ++i) Vc[i] = LDB(rv, ((4 * i + r) * DH + 8 * c) * 2, AUX_STREAM);
  PIN();
  const GAS u64* hp = (const GAS u64*)(ws + WS_H) + ((jf * 8 + g) * 8 + (lane >> 3)) * 16 + (lane & 7);
  PIN();
  const u64 tagE = E + 1;
  const u32 tlo = (u32)tagE, thi8 = (u32)(tagE >> 32) & 0xffu;
  asm volatile("" :: "v"(tch), "v"(tch2), "v"(tch3));
  asm volatile("" :: "v"(cpf0));
#pragma unroll
  for (int i = 0; i < 8; ++i) *(u32x4*)(lds + L_KT + voff(32 * wv + 4 * i + r, c)) = Kf[i];
  PIN();
  if (lane == 0) __hip_atomic_fetch_add((u32*)(lds + L_CNT + 16), 1u, __ATOMIC_RELAXED, __HIP_MEMORY_SCOPE_WORKGROUP);
  u64 hg = ld_agent(hp);
  PIN();
#pragma unroll
  for (int i = 0; i < 8; ++i) Kf[i] = *(const u32x4*)(lds + L_KT + voff(32 * wv + (i >> 2) * 16 + c, 4 * (i & 3) + r));
  PIN();
#pragma unroll
  for (int i = 0; i < 8; ++i) *(u32x4*)(lds + L_VT + voff(32 * wv + 4 * i + r, c)) = Vc[i];
  u32x4 Vf[8];
  {
    const int q4 = c >> 2, p4 = c & 3;
#pragma unroll
    for (int n = 0; n < 8; ++n) {
      const u32x2 w0 = __builtin_bit_cast(u32x2, __builtin_amdgcn_ds_read_tr16_b64_v4i16((LAS s16x4*)(lds + L_VT + voff(32 * wv + 4 * r + q4, 2 * n + (p4 >> 1)) + 8 * (p4 & 1))));
      const u32x2 w1 = __builtin_bit_cast(u32x2, __builtin_amdgcn_ds_read_tr16_b64_v4i16((LAS s16x4*)(lds + L_VT + voff(32 * wv + 16 + 4 * r + q4, 2 * n + (p4 >> 1)) + 8 * (p4 & 1))));
      Vf[n] = u32x4{w0.x, w0.y, w1.x, w1.y};
    }
  }
  float y[8];
  {
    if (has_ffn) {
      for (u32 spins = 0; !__all(bad40(hg, tlo, thi8) == 0) && spins < SPIN_CAP; ++spins) hg = ld_agent(hp);
    }
    float hv = has_ffn ? val40((u32)(hg >> 32)) : 0.f;
    hv += dpp<XOR8>(hv); hv = xrow_sum(hv);
    hv = fmaxf(bfr(hv), 0.f);
    const float h0 = __shfl(hv, r), h1 = __shfl(hv, r + 4);
#pragma unroll
    for (int k = 0; k < 4; ++k) {
      y[2 * k] = fmaf(h0, lo16(W2[0][k]), h1 * lo16(W2[1][k]));
      y[2 * k + 1] = fmaf(h0, hi16(W2[0][k]), h1 * hi16(W2[1][k]));
    }
  }
  PIN();
  lds_spin_ge(lds + L_CNT + 4, 4u);
  {
    f32x4 a0 = {0.f, 0.f, 0.f, 0.f}, a1 = {0.f, 0.f, 0.f, 0.f};
#pragma unroll
    for (int i = 0; i < 4; ++i) {
      const u32x4 qf = *(const u32x4*)(lds + L_QS + (c & 3) * 256 + 64 * i + 16 * r);
      a0 = mfma16(Kf[i], qf, a0);
      a1 = mfma16(Kf[4 + i], qf, a1);
    }
    float sc[8];
#pragma unroll
    for (int i = 0; i < 4; ++i) { sc[i] = bfr(bfr(a0[i]) * 0.08838834764831845f); sc[4 + i] = bfr(bfr(a1[i]) * 0.08838834764831845f); }
    const float mw = xrow_max(fmaxf(fmaxf(fmaxf(sc[0], sc[1]), fmaxf(sc[2], sc[3])), fmaxf(fmaxf(sc[4], sc[5]), fmaxf(sc[6], sc[7]))));
#pragma unroll
    for (int i = 0; i < 8; ++i) sc[i] = __builtin_amdgcn_exp2f((sc[i] - mw) * LOG2E);
    const u32x4 pf = {pk2(sc[0], sc[1]), pk2(sc[2], sc[3]), pk2(sc[4], sc[5]), pk2(sc[6], sc[7])};
    const u32x4 ones = {0x3f803f80u, 0x3f803f80u, 0x3f803f80u, 0x3f803f80u};
    const f32x4 z = {0.f, 0.f, 0.f, 0.f};
    float ow[8];
#pragma unroll
    for (int n = 0; n < 8; ++n) ow[n] = sel4(mfma16(pf, Vf[n], z), r);
    const f32x4 lac = mfma16(pf, ones, z);
    *(f32x4*)(lds + L_OW + ((wv * 4 + r) * 16 + c) * 32) = f32x4{ow[0], ow[1], ow[2], ow[3]};
    *(f32x4*)(lds + L_OW + ((wv * 4 + r) * 16 + c) * 32 + 16) = f32x4{ow[4], ow[5], ow[6], ow[7]};
    if (lane < 4) { *(float*)(lds + L_WML + (lane * 4 + wv) * 4) = mw; *(float*)(lds + L_WML + 64 + (lane * 4 + wv) * 4) = sel4(lac, lane); }
  }
  __syncthreads();
  {
    const f32x4 m4 = *(const f32x4*)(lds + L_WML + r * 16), l4 = *(const f32x4*)(lds + L_WML + 64 + r * 16);
    f32x2 pv[4];
#pragma unroll
    for (int w = 0; w < 4; ++w) pv[w] = *(const f32x2*)(lds + L_OW + ((w * 4 + r) * 16 + c) * 32 + wv * 8);
    const float M = fmaxf(fmaxf(m4.x, m4.y), fmaxf(m4.z, m4.w));
    float e[4];
#pragma unroll
    for (int w = 0; w < 4; ++w) e[w] = __builtin_amdgcn_exp2f((m4[w] - M) * LOG2E);
    const float L = fmaf(e[0], l4.x, fmaf(e[1], l4.y, fmaf(e[2], l4.z, e[3] * l4.w)));
    const float rl = __builtin_amdgcn_rcpf(L);
    const float x0 = fmaf(e[0], pv[0].x, fmaf(e[1], pv[1].x, fmaf(e[2], pv[2].x, e[3] * pv[3].x))) * rl;
    const float x1 = fmaf(e[0], pv[0].y, fmaf(e[1], pv[1].y, fmaf(e[2], pv[2].y, e[3] * pv[3].y))) * rl;
    const float y0 = dpp<XOR1>(x0), y1 = dpp<XOR1>(x1);
    const bool odd = c & 1;
    const u32 pr = odd ? pk2(y1, x1) : pk2(x0, y0);
    const u64 lg = pack40(fmaf(M, LOG2E, __builtin_amdgcn_logf(L)), tlo, thi8);
    const __amdgpu_buffer_rsrc_t ro = __builtin_amdgcn_make_buffer_rsrc((void*)(ws_ + WS_O), 0, (int)(256 * 512 * 8), 0x00020000);
    __builtin_amdgcn_raw_buffer_store_b128(u32x4{pr, tlo, (u32)lg, (u32)(lg >> 32)}, ro, ((g * 32 + r * 8 + 2 * wv + (c & 1)) * 32 + j) * 128 + (c >> 1) * 16, 0, 16);
  }
  PIN();
  {
    const __amdgpu_buffer_rsrc_t ro = __builtin_amdgcn_make_buffer_rsrc((void*)(ws_ + WS_O), 0, (int)(256 * 512 * 8), 0x00020000);
    const int off = (g * 32 + j) * 4096 + t * 16;
    u32x4 og;
    f32x2 wf[16];
#pragma unroll
    for (int i = 0; i < 4; ++i)
#pragma unroll
      for (int k = 0; k < 4; ++k) { wf[4 * i + k] = f32x2{lo16(Wo[i][k]), hi16(Wo[i][k])}; asm volatile("" : "+v"(wf[4 * i + k])); }
    __builtin_amdgcn_s_sleep(POLL_DELAY);
    for (u32 spins = 0;; ++spins) {
      asm volatile("" ::: "memory");
      og = __builtin_amdgcn_raw_buffer_load_b128(ro, off, 0, 16);
      const u32 bad = (og.y ^ tlo) | (og.z ^ tlo) | ((og.w ^ thi8) & 0xffu);
      if (__all(bad == 0) || spins >= SPIN_CAP) break;
    }
    const float lv = val40(og.w);
    float Mw = lv;
    Mw = fmaxf(Mw, dpp<XOR8>(Mw)); Mw = xrow_max(Mw);
    const float mine = __builtin_amdgcn_exp2f(lv - Mw);
    float Ww = mine;
    Ww += dpp<XOR8>(Ww); Ww = xrow_sum(Ww);
    float v = tr32(mine * lo16(og.x), mine * hi16(og.x));
    { auto tt = __builtin_amdgcn_permlane16_swap(__float_as_uint(v), __float_as_uint(v), false, false); v = __uint_as_float(tt[0]) + __uint_as_float(tt[1]); }
    v += dpp<XOR8>(v);
    if ((lane & 24) == 0) *(float*)(lds + L_ORED + ((2 * (lane & 7) + (lane >> 5)) * 4 + wv) * 4) = v;
    if (lane == 8) { *(float*)(lds + L_HV + wv * 4) = Mw; *(float*)(lds + L_HV + 16 + wv * 4) = Ww; }
    __syncthreads();
    const f32x4 m4 = *(const f32x4*)(lds + L_HV), w4 = *(const f32x4*)(lds + L_HV + 16);
    const float Mx = fmaxf(fmaxf(m4.x, m4.y), fmaxf(m4.z, m4.w));
    f32x4 e4;
#pragma unroll
    for (int w = 0; w < 4; ++w) e4[w] = __builtin_amdgcn_exp2f(m4[w] - Mx);
    const float rL = __builtin_amdgcn_rcpf(fmaf(e4.x, w4.x, fmaf(e4.y, w4.y, fmaf(e4.z, w4.z, e4.w * w4.w))));
#pragma unroll
    for (int i = 0; i < 4; ++i) {
      const f32x4 a0 = *(const f32x4*)(lds + L_ORED + (r + 4 * i) * 16);
      const float o = bfr(fmaf(e4.x, a0.x, fmaf(e4.y, a0.y, fmaf(e4.z, a0.z, e4.w * a0.w))) * rL);
#pragma unroll
      for (int k = 0; k < 4; ++k) { y[2 * k] = fmaf(o, wf[4 * i + k].x, y[2 * k]); y[2 * k + 1] = fmaf(o, wf[4 * i + k].y, y[2 * k + 1]); }
    }
    const float a0 = tr32(y[0], y[4]), a1 = tr32(y[1], y[5]), a2 = tr32(y[2], y[6]), a3 = tr32(y[3], y[7]);
    const float b0 = tr16(a0, a2), b1 = tr16(a1, a3);
    const u64 g0 = pack40(b0, tlo, thi8), g1 = pack40(b1, tlo, thi8);
    const __amdgpu_buffer_rsrc_t rf = __builtin_amdgcn_make_buffer_rsrc((void*)(ws_ + WS_F), 0, (int)(256 * 512 * 8), 0x00020000);
    const int offf = (((16 * wv + c) * 256 + b) * CPR + (lane >> 5) * 4 + ((lane >> 4) & 1) * 2) * 8;
    __builtin_amdgcn_raw_buffer_store_b128(u32x4{(u32)g0, (u32)(g0 >> 32), (u32)g1, (u32)(g1 >> 32)}, rf, offf, 0, 16);
  }
  PIN();
  if (b >= NRED) return;
  {
    const __amdgpu_buffer_rsrc_t rs = __builtin_amdgcn_make_buffer_rsrc((void*)(ws_ + WS_F), 0, (int)(256 * 512 * 8), 0x00020000);
    const int off = b * 16384 + tid * 16;
    const u32 xo = x[b * CPR + (tid & 7)];
    __builtin_amdgcn_s_sleep(POLL_DELAY);
    u32x4 fg[4];
    for (u32 spins = 0;; ++spins) {
      asm volatile("" ::: "memory");
#pragma unroll
      for (int k = 0; k < 4; ++k) fg[k] = __builtin_amdgcn_raw_buffer_load_b128(rs, off + 4096 * k, 0, 16);
      u32 bad = 0;
#pragma unroll
      for (int k = 0; k < 4; ++k) bad |= (fg[k].x ^ tlo) | ((fg[k].y ^ thi8) & 0xffu) | (fg[k].z ^ tlo) | ((fg[k].w ^ thi8) & 0xffu);
      if (__all(bad == 0) || spins >= SPIN_CAP) break;
    }
    const float s0 = (val40(fg[0].y) + val40(fg[1].y)) + (val40(fg[2].y) + val40(fg[3].y));
    const float s1 = (val40(fg[0].w) + val40(fg[1].w)) + (val40(fg[2].w) + val40(fg[3].w));
    float s = tr32(s0, s1);
    { auto tt = __builtin_amdgcn_permlane16_swap(__float_as_uint(s), __float_as_uint(s), false, false); s = __uint_as_float(tt[0]) + __uint_as_float(tt[1]); }
    s += dpp<XOR8>(s);
    s += __builtin_bit_cast(float, __builtin_amdgcn_ds_swizzle(__builtin_bit_cast(int, s), 0x101F));
    if ((lane & 28) == 0) *(float*)(lds + L_FRED + ((2 * (lane & 3) + (lane >> 5)) * 4 + wave) * 4) = s;
    __syncthreads();
    if (tid < CPR) {
      const f32x4 a = *(const f32x4*)(lds + L_FRED + tid * 16);
      ((GAS u16*)out_)[b * CPR + tid] = (u16)bfbits(lo16(xo) + ((a.x + a.y) + (a.z + a.w)));
    }
    if (b == 0 && tid == 0) st_agent((GAS u64*)ws, tagE);
  }
}

}

extern "C" void kernel_launch(void* const* d_in, const int* in_sizes, int n_in,
                              void* d_out, int out_size, void* d_ws, size_t ws_size,
                              hipStream_t stream) {
  const u16* x = (const u16*)d_in[0];
  const u16* kc = (const u16*)d_in[1];
  const u16* vc = (const u16*)d_in[2];
  const u16* wq = (const u16*)d_in[3];
  const u16* wo = (const u16*)d_in[4];
  const u16* w1 = (const u16*)d_in[6];
  const u16* w2 = (const u16*)d_in[7];
  const int Sk = in_sizes[1] / (HKV * DH);
  fused_decode_kernel<<<NWG, NT, 0, stream>>>(x, kc, vc, wq, wo, w1, w2, (u16*)d_out, (unsigned char*)d_ws, Sk);
}
```
